```python
import torch

def _softmax(x, axis=-1):
    return torch.softmax(x, dim=axis)
def _gelu(x):
    return torch.nn.functional.gelu(x, approximate="tanh")
def _silu(x):
    return torch.nn.functional.silu(x)
def _sigmoid(x):
    return torch.sigmoid(x)
def _layernorm(x, axis=-1, eps=1e-5):
    m = x.mean(dim=axis, keepdim=True)
    v = x.var(dim=axis, keepdim=True, unbiased=False)
    return (x - m) / torch.sqrt(v + eps)
def _scatter_add(tgt, idx, src, axis=0):
    return tgt.clone().index_add_(axis, idx, src)
def _segment_sum(src, seg, n):
    out = torch.zeros((n,) + src.shape[1:], dtype=src.dtype)
    return out.index_add_(0, seg, src)
def _pad(x, pw):
    flat = tuple(v for pair in reversed(pw) for v in pair)
    return torch.nn.functional.pad(x, flat)

kernel_name = 'concat_heads__grouped_kv_mqa__compressed_kv_cla__prompt'

def setup_inputs():

    _w = (lambda a: (lambda t: t.to(torch.bfloat16) if t.is_floating_point() else t)(torch.as_tensor(a)))
    def _A_setup():
        B, S, D, H = 1, 2048, 1024, 32
        Dh = D // H
        return {
            "x": _w((lambda *s: torch.randn(*s, dtype=torch.bfloat16) * 0.05)(B, S, D)),
            "wq": _w((lambda *s: torch.randn(*s, dtype=torch.bfloat16) * 0.8)(D, H * Dh)),
            "wk": _w((lambda *s: torch.randn(*s, dtype=torch.bfloat16) * 0.8)(D, Dh)),
            "wv": _w((lambda *s: torch.randn(*s, dtype=torch.bfloat16) * 0.05)(D, Dh)),
            "wo": _w((lambda *s: torch.randn(*s, dtype=torch.bfloat16) * 0.05)(H * Dh, D)),
        }
    _A_in = _A_setup()

    def _B_setup():
        B, H, Sq, Sk, D, M = 1, 32, 2048, 2048, 128, 1024
        Dc = 4 * D
        return {
            "x": _w((lambda *s: torch.randn(*s, dtype=torch.bfloat16) * 1.6)(B, Sq, M)),
            "c_kv": _w((lambda *s: torch.randn(*s, dtype=torch.bfloat16) * 0.2)(B, Sk, Dc)),
            "w_q": _w((lambda *s: torch.randn(*s, dtype=torch.bfloat16) * 6.4)(M, H, D) * (M ** -0.5)),
            "w_uk": _w((lambda *s: torch.randn(*s, dtype=torch.bfloat16) * 6.4)(Dc, H, D) * (Dc ** -0.5)),
            "w_uv": _w((lambda *s: torch.randn(*s, dtype=torch.bfloat16) * 0.2)(Dc, H, D) * (Dc ** -0.5)),
            "w_o": _w((lambda *s: torch.randn(*s, dtype=torch.bfloat16) * 0.2)(H, D, M) * ((H * D) ** -0.5)),
        }
    _B_in = _B_setup()

    return {
        "A_x": _A_in["x"],
        "A_wq": _A_in["wq"],
        "A_wk": _A_in["wk"],
        "A_wv": _A_in["wv"],
        "A_wo": _A_in["wo"],
        "B_x": _B_in["x"],
        "B_c_kv": _B_in["c_kv"],
        "B_w_q": _B_in["w_q"],
        "B_w_uk": _B_in["w_uk"],
        "B_w_uv": _B_in["w_uv"],
        "B_w_o": _B_in["w_o"],
    }

def reference(A_x, A_wq, A_wk, A_wv, A_wo, B_x, B_c_kv, B_w_q, B_w_uk, B_w_uv, B_w_o):
    def _A_ref(x, wq, wk, wv, wo):
        B, S, D = x.shape
        H = 32
        Dh = D // H
        q = (lambda x, *d: x.permute(*d))((x @ wq).reshape(B, S, H, Dh), 0, 2, 1, 3)
        k = x @ wk
        v = x @ wv
        scores = torch.einsum("bhqd,bkd->bhqk", q, k) * (Dh ** -0.5)
        p = _softmax(scores, axis=-1)
        o = torch.einsum("bhqk,bkd->bhqd", p, v)
        return (lambda x, *d: x.permute(*d))(o, 0, 2, 1, 3).reshape(B, S, H * Dh) @ wo

    def _B_ref(x, c_kv, w_q, w_uk, w_uv, w_o):
        B, Sq, M = x.shape[0], x.shape[1], x.shape[2]
        Sk = c_kv.shape[1]
        H, D = w_q.shape[1], w_q.shape[2]
        q = torch.einsum("bqm,mhd->bhqd", x, w_q)
        k = torch.einsum("bkc,chd->bhkd", c_kv, w_uk)
        v = torch.einsum("bkc,chd->bhkd", c_kv, w_uv)
        scores = torch.einsum("bhqd,bhkd->bhqk", q, k) * (D ** -0.5)
        off = Sk - Sq
        qi = torch.arange(Sq)[:, None]
        kj = torch.arange(Sk)[None, :]
        cmask = kj <= (qi + off)
        scores = torch.where(cmask[None, None], scores, -1e9)
        p = _softmax(scores, axis=-1)
        ctx = torch.einsum("bhqk,bhkd->bhqd", p, v)
        return torch.einsum("bhqd,hdm->bqm", ctx, w_o)

    a = _A_ref(A_x, A_wq, A_wk, A_wv, A_wo)
    b = _B_ref(B_x, B_c_kv, B_w_q, B_w_uk, B_w_uv, B_w_o)
    return torch.concatenate([a, b], axis=-1)
```

```cpp
#include <hip/hip_runtime.h>
#include <hip/hip_bf16.h>

typedef unsigned short u16;
typedef short bf16x8 __attribute__((ext_vector_type(8)));
typedef short bf16x4 __attribute__((ext_vector_type(4)));
typedef float f32x16 __attribute__((ext_vector_type(16)));
typedef float f32x4 __attribute__((ext_vector_type(4)));
typedef float f32x2 __attribute__((ext_vector_type(2)));
typedef __bf16 bf16x2_t __attribute__((ext_vector_type(2)));
typedef unsigned u32x4 __attribute__((ext_vector_type(4)));
#define LAS __attribute__((address_space(3)))
#define GAS __attribute__((address_space(1)))
#define DI __device__ __forceinline__

DI f32x16 mfma32(bf16x8 a, bf16x8 b, f32x16 c) { return __builtin_amdgcn_mfma_f32_32x32x16_bf16(a, b, c, 0, 0, 0); }
DI unsigned cvtpk(float lo, float hi) { f32x2 v = {lo, hi}; bf16x2_t b = __builtin_convertvector(v, bf16x2_t); return __builtin_bit_cast(unsigned, b); }
DI void glds16(const void* g, LAS void* l) { __builtin_amdgcn_global_load_lds((const GAS unsigned*)g, (LAS unsigned*)l, 16, 0, 0); }
DI bf16x8 lds_b128(LAS const char* p) { return *(LAS const bf16x8*)p; }
DI bf16x4 lds_tr(LAS const char* p) { return __builtin_amdgcn_ds_read_tr16_b64_v4i16((LAS bf16x4*)p); }
DI bf16x8 cat8(bf16x4 a, bf16x4 b) { return (bf16x8){a[0], a[1], a[2], a[3], b[0], b[1], b[2], b[3]}; }
DI float swap32_max(float x) { auto rr = __builtin_amdgcn_permlane32_swap(__float_as_uint(x), __float_as_uint(x), false, false); return fmaxf(__uint_as_float(rr[0]), __uint_as_float(rr[1])); }
DI float swap32_add(float x) { auto rr = __builtin_amdgcn_permlane32_swap(__float_as_uint(x), __float_as_uint(x), false, false); return __uint_as_float(rr[0]) + __uint_as_float(rr[1]); }
#define WAIT_BAR(N) asm volatile("s_waitcnt vmcnt(" #N ") lgkmcnt(0)\n\ts_barrier" ::: "memory")
#define WAIT_BAR_I(N) asm volatile("s_waitcnt vmcnt(%0) lgkmcnt(0)\n\ts_barrier" :: "i"(N) : "memory")

DI u32x4 pack8(const f32x16& a, int b, float sc) {
    u32x4 w; w.x = cvtpk(a[b + 0] * sc, a[b + 1] * sc); w.y = cvtpk(a[b + 2] * sc, a[b + 3] * sc);
    w.z = cvtpk(a[b + 4] * sc, a[b + 5] * sc); w.w = cvtpk(a[b + 6] * sc, a[b + 7] * sc); return w;
}

constexpr int SEQ = 2048, NTB = SEQ / 32;
constexpr float LOG2E = 1.4426950408889634f;
constexpr float CA = 0.17677669529663687f * LOG2E;
constexpr float CB = 0.08838834764831845f * LOG2E;
constexpr float SM_BIAS = 64.f;

constexpr size_t WS_QB = 0, WS_KB = WS_QB + (16u << 20), WS_VB = WS_KB + (16u << 20), WS_QA = WS_VB + (16u << 20),
                 WS_KA = WS_QA + (4u << 20), WS_VA = WS_KA + (128u << 10), WS_CTX = WS_VA + (128u << 10), WS_OA = WS_CTX + (16u << 20),
                 WS_PART = WS_OA + (4u << 20), WS_FLAG = WS_PART + (8u << 20), WS_END = WS_FLAG + 128 * 64;

struct P1 { const u16 *xA, *wqA, *wkA, *wvA, *xB, *cKV, *wqB, *wukB, *wuvB; char* ws; };

template <bool OT>
DI void big_unit(LAS char* lds, const char* Ab, unsigned lda_b, const char* Wb, unsigned ldw_b, int nt, f32x16 (&acc)[4][2], int wid, int lane) {
    const int wr = wid >> 2, wc = wid & 3, r = lane & 31, h = lane >> 5;
    unsigned voA[4], voW[4];
#pragma unroll
    for (int i = 0; i < 4; ++i) {
        const int row = (wid * 4 + i) * 8 + (lane >> 3), ch = (lane & 7) ^ ((row >> 1) & 7);
        voA[i] = (unsigned)row * lda_b + ch * 16;
        const int k = ((wid & 1) * 4 + i) * 8 + (lane >> 3), slot = lane & 7;
        const int col = 64 * (wid >> 1) + 32 * ((slot >> 2) ^ ((k >> 1) & 1)) + 8 * (slot & 3);
        voW[i] = (unsigned)k * ldw_b + col * 2;
    }
    int aoff[4], boff[2];
#pragma unroll
    for (int s = 0; s < 4; ++s) aoff[s] = (128 * wr + r) * 128 + (((2 * s + h) ^ ((r >> 1) & 7)) << 4);
    { const int blk = (lane >> 4) & 1, q = (lane & 15) >> 2, p = lane & 3;
#pragma unroll
      for (int n = 0; n < 2; ++n) boff[n] = 32768 + wc * 8192 + (8 * h + q) * 128 + 64 * (n ^ (q >> 1)) + 32 * blk + 8 * p; }
#pragma unroll
    for (int m = 0; m < 4; ++m) { acc[m][0] = f32x16{}; acc[m][1] = f32x16{}; }
    auto stage = [&](int buf, int t) {
        const char* a = Ab + (size_t)t * 128; const char* w = Wb + (size_t)t * 64 * ldw_b;
        LAS char* base = lds + buf * 65536;
#pragma unroll
        for (int i = 0; i < 4; ++i) glds16(a + voA[i], base + (wid * 4 + i) * 1024);
#pragma unroll
        for (int i = 0; i < 4; ++i) glds16(w + voW[i], base + 32768 + (wid >> 1) * 8192 + ((wid & 1) * 4 + i) * 1024);
    };
    auto compute = [&](int buf) {
        LAS const char* sa = lds + buf * 65536;
#pragma unroll
        for (int s = 0; s < 4; ++s) {
            bf16x8 af[4], wf[2];
#pragma unroll
            for (int n = 0; n < 2; ++n) wf[n] = cat8(lds_tr(sa + boff[n] + s * 2048), lds_tr(sa + boff[n] + s * 2048 + 512));
#pragma unroll
            for (int m = 0; m < 4; ++m) af[m] = lds_b128(sa + aoff[s] + m * 4096);
#pragma unroll
            for (int m = 0; m < 4; ++m)
#pragma unroll
                for (int n = 0; n < 2; ++n) acc[m][n] = OT ? mfma32(wf[n], af[m], acc[m][n]) : mfma32(af[m], wf[n], acc[m][n]);
        }
    };
    stage(0, 0);
    for (int t = 0; t < nt; t += 2) {
        WAIT_BAR(0);
        stage(1, t + 1);
        compute(0);
        WAIT_BAR(0);
        if (t + 2 < nt) stage(0, t + 2);
        compute(1);
    }
    WAIT_BAR(0);
}

template <bool OT>
DI void small_loop(LAS char* lds, const char* xa, const char* wsrc, unsigned wstep, const unsigned (&voX)[2], f32x16& acc, int wid, int lane) {
    constexpr int NS = 5, STG = 24576, NT = 16;
    const int wm = wid >> 1, wn = wid & 1, r = lane & 31, h = lane >> 5;
    int aoff[4];
#pragma unroll
    for (int s = 0; s < 4; ++s) aoff[s] = (32 * wm + r) * 128 + (((2 * s + h) ^ ((r >> 1) & 7)) << 4);
    const int blk = (lane >> 4) & 1, q = (lane & 15) >> 2, p = lane & 3;
    const int boff = 16384 + (8 * h + q) * 128 + 64 * (wn ^ (q >> 1)) + 32 * blk + 8 * p;
    auto stage = [&](int slot, int t) {
        LAS char* base = lds + slot * STG;
        glds16(xa + (size_t)t * 128 + voX[0], base + (wid * 2) * 1024);
        glds16(xa + (size_t)t * 128 + voX[1], base + (wid * 2 + 1) * 1024);
        glds16(wsrc + (size_t)t * wstep, base + 16384 + wid * 1024);
    };
    acc = f32x16{};
#pragma unroll
    for (int t = 0; t < NS - 1; ++t) stage(t, t);
    int slot = 0;
    for (int t = 0; t < NT; ++t) {
        if (t + NS - 1 <= NT) { WAIT_BAR(9); } else { WAIT_BAR(0); }
        if (t + NS - 1 < NT) { int sl = slot + NS - 1; if (sl >= NS) sl -= NS; stage(sl, t + NS - 1); }
        LAS const char* sa = lds + slot * STG;
#pragma unroll
        for (int s = 0; s < 4; ++s) {
            const bf16x8 wf = cat8(lds_tr(sa + boff + s * 2048), lds_tr(sa + boff + s * 2048 + 512));
            const bf16x8 af = lds_b128(sa + aoff[s]);
            acc = OT ? mfma32(wf, af, acc) : mfma32(af, wf, acc);
        }
        if (++slot == NS) slot = 0;
    }
    WAIT_BAR(0);
}

DI void small_unit(LAS char* lds, const P1& p, int pm, int cg, int wid, int lane) {
    const int wm = wid >> 1, wn = wid & 1;
    unsigned voX[2];
#pragma unroll
    for (int i = 0; i < 2; ++i) { const int row = (wid * 2 + i) * 8 + (lane >> 3), ch = (lane & 7) ^ ((row >> 1) & 7); voX[i] = (unsigned)row * 2048u + ch * 16; }
    const char* xa = (const char*)p.xA + (size_t)pm * 128 * 2048;
    const int k = wid * 8 + (lane >> 3), slot = lane & 7, half = (slot >> 2) ^ ((k >> 1) & 1);
    const char* wsrc; unsigned wstep;
    if (cg < 16) { wsrc = (const char*)p.wqA + (size_t)k * 2048 + (64 * cg + 32 * half + 8 * (slot & 3)) * 2; wstep = 64 * 2048; }
    else { wsrc = (const char*)(half ? p.wvA : p.wkA) + (size_t)k * 64 + (slot & 3) * 16; wstep = 64 * 64; }
    f32x16 acc;
    const int tb = pm * 4 + wm;
    if (cg == 16 && wn == 1) {
        small_loop<false>(lds, xa, wsrc, wstep, voX, acc, wid, lane);
        char* dst = p.ws + WS_VA + (size_t)(tb * 2) * 1024 + lane * 16;
        *(u32x4*)dst = pack8(acc, 0, 1.f); *(u32x4*)(dst + 1024) = pack8(acc, 8, 1.f);
    } else {
        small_loop<true>(lds, xa, wsrc, wstep, voX, acc, wid, lane);
        const float sc = cg < 16 ? CA : 1.f;
        char* dst = cg < 16 ? p.ws + WS_QA + (size_t)(((2 * cg + wn) * NTB + tb) * 2) * 1024 + lane * 16 : p.ws + WS_KA + (size_t)(tb * 2) * 1024 + lane * 16;
        *(u32x4*)dst = pack8(acc, 0, sc); *(u32x4*)(dst + 1024) = pack8(acc, 8, sc);
    }
}

__global__ __launch_bounds__(512, 2) void k_proj(P1 p) {
    extern __shared__ __attribute__((aligned(16))) char smem[];
    LAS char* lds = (LAS char*)smem;
    const int tid = threadIdx.x, wid = __builtin_amdgcn_readfirstlane(tid >> 6), lane = tid & 63;
    const int bid = blockIdx.x, xcd = bid & 7, idx = bid >> 3;
    if (bid == 0 && tid < 128) ((unsigned*)(p.ws + WS_FLAG))[tid * 16] = 0u;
    const int wr = wid >> 2, wc = wid & 3;
    const int pm = idx & 7, pn = 2 * xcd + ((idx >> 3) & 1), kind = idx >> 4;
    f32x16 acc[4][2];
    auto store_f = [&](char* base, float sc) {
#pragma unroll
        for (int m = 0; m < 4; ++m)
#pragma unroll
            for (int n = 0; n < 2; ++n) { const int tb = pm * 8 + wr * 4 + m, fb = pn * 8 + wc * 2 + n, hd = fb >> 2, db = fb & 3;
                char* dst = base + (size_t)((hd * NTB + tb) * 8 + 2 * db) * 1024 + lane * 16;
                *(u32x4*)dst = pack8(acc[m][n], 0, sc); *(u32x4*)(dst + 1024) = pack8(acc[m][n], 8, sc); }
    };
    if (kind == 0) {
        big_unit<true>(lds, (const char*)p.xB + (size_t)pm * 256 * 2048, 2048, (const char*)p.wqB + pn * 512, 8192, 16, acc, wid, lane);
        store_f(p.ws + WS_QB, CB);
    } else {
        big_unit<true>(lds, (const char*)p.cKV + (size_t)pm * 256 * 1024, 1024, (const char*)p.wukB + pn * 512, 8192, 8, acc, wid, lane);
        store_f(p.ws + WS_KB, 1.f);
        big_unit<false>(lds, (const char*)p.cKV + (size_t)pm * 256 * 1024, 1024, (const char*)p.wuvB + pn * 512, 8192, 8, acc, wid, lane);
#pragma unroll
        for (int m = 0; m < 4; ++m)
#pragma unroll
            for (int n = 0; n < 2; ++n) { const int tb = pm * 8 + wr * 4 + m, fb = pn * 8 + wc * 2 + n, hd = fb >> 2, db = fb & 3;
                char* dst = p.ws + WS_VB + (size_t)((hd * NTB + tb) * 8 + db) * 1024 + lane * 16;
                *(u32x4*)dst = pack8(acc[m][n], 0, 1.f); *(u32x4*)(dst + 4096) = pack8(acc[m][n], 8, 1.f); }
    }
    small_unit(lds, p, (bid >> 3) & 15, xcd + 8 * (bid >> 7), wid, lane);
    if (kind == 0 && (idx & 8) == 0 && xcd < 2) small_unit(lds, p, xcd * 8 + pm, 16, wid, lane);
}

struct P2 { char* ws; };

DI void exp16(const f32x16& s, unsigned (&pk)[8], float& t0, float& t1, unsigned& orv) {
#pragma unroll
    for (int i = 0; i < 8; ++i) {
        const float a = __builtin_amdgcn_exp2f(s[2 * i]), b = __builtin_amdgcn_exp2f(s[2 * i + 1]);
        t0 += a; t1 += b; pk[i] = cvtpk(a, b); orv |= pk[i];
    }
}
DI float max16(const f32x16& s) {
    float m = fmaxf(s[0], s[1]);
#pragma unroll
    for (int i = 2; i < 16; ++i) m = fmaxf(m, s[i]);
    return m;
}
DI void diag_mask(f32x16& s, int r, int h) {
#pragma unroll
    for (int i = 0; i < 16; ++i) { const int kr = (i & 3) + 8 * (i >> 2) + 4 * h; if (kr > r) s[i] = -__builtin_inff(); }
}
DI bf16x8 pfrag(const unsigned (&pk)[8], int b) { u32x4 w = {pk[b], pk[b + 1], pk[b + 2], pk[b + 3]}; return __builtin_bit_cast(bf16x8, w); }

DI void attnB(LAS char* lds, char* ws, int hd, int xb, int wid, int lane) {
    const int r = lane & 31, h = lane >> 5;
    const int tbq = 4 * xb + wid;
    const char* kbase = ws + WS_KB + (size_t)hd * NTB * 8192 + wid * 4096 + lane * 16;
    const char* vbase = ws + WS_VB + (size_t)hd * NTB * 8192 + wid * 4096 + lane * 16;
    auto stage = [&](int buf, int t) {
        LAS char* b = lds + buf * 32768 + wid * 4096;
#pragma unroll
        for (int i = 0; i < 4; ++i) glds16(kbase + (size_t)t * 16384 + i * 1024, b + i * 1024);
#pragma unroll
        for (int i = 0; i < 4; ++i) glds16(vbase + (size_t)t * 16384 + i * 1024, b + 16384 + i * 1024);
    };
    stage(0, 0);
    bf16x8 qf[8];
#pragma unroll
    for (int s = 0; s < 8; ++s) qf[s] = *(const bf16x8*)(ws + WS_QB + (size_t)((hd * NTB + tbq) * 8 + s) * 1024 + lane * 16);
    f32x16 o[4], negm; float l = 0.f;
#pragma unroll
    for (int d = 0; d < 4; ++d) o[d] = f32x16{};
    const int NT = 2 * (xb + 1);
    auto rescale = [&](float mx) {
        const float delta = fmaxf(mx + SM_BIAS, 0.f), alpha = __builtin_amdgcn_exp2f(-delta);
        l *= alpha;
#pragma unroll
        for (int d = 0; d < 4; ++d) o[d] *= alpha;
        negm -= delta;
        return delta;
    };
    auto tile64 = [&](int buf) {
        LAS const char* kb = lds + buf * 32768 + lane * 16;
        f32x16 s0 = negm, s1 = negm;
#pragma unroll
        for (int s = 0; s < 8; ++s) { s0 = mfma32(lds_b128(kb + s * 1024), qf[s], s0); s1 = mfma32(lds_b128(kb + 8192 + s * 1024), qf[s], s1); }
        unsigned p0[8], p1[8], orv = 0; float t0 = 0.f, t1 = 0.f;
        exp16(s0, p0, t0, t1, orv); exp16(s1, p1, t0, t1, orv);
        if (__builtin_expect(__any((orv & 0x40004000u) != 0u), 0)) {
            const float delta = rescale(swap32_max(fmaxf(max16(s0), max16(s1))));
            s0 -= delta; s1 -= delta; t0 = 0.f; t1 = 0.f;
            exp16(s0, p0, t0, t1, orv); exp16(s1, p1, t0, t1, orv);
        }
        l += t0 + t1;
#pragma unroll
        for (int ks = 0; ks < 2; ++ks)
#pragma unroll
            for (int d = 0; d < 4; ++d) o[d] = mfma32(lds_b128(kb + 16384 + (ks * 4 + d) * 1024), pfrag(p0, 4 * ks), o[d]);
#pragma unroll
        for (int ks = 0; ks < 2; ++ks)
#pragma unroll
            for (int d = 0; d < 4; ++d) o[d] = mfma32(lds_b128(kb + 16384 + 8192 + (ks * 4 + d) * 1024), pfrag(p1, 4 * ks), o[d]);
    };
    auto tile32 = [&](int buf, int u, bool diag) {
        LAS const char* kb = lds + buf * 32768 + u * 8192 + lane * 16;
        f32x16 s0 = negm;
#pragma unroll
        for (int s = 0; s < 8; ++s) s0 = mfma32(lds_b128(kb + s * 1024), qf[s], s0);
        if (diag) diag_mask(s0, r, h);
        unsigned p0[8], orv = 0; float t0 = 0.f, t1 = 0.f;
        exp16(s0, p0, t0, t1, orv);
        if (__builtin_expect(__any((orv & 0x40004000u) != 0u), 0)) {
            const float delta = rescale(swap32_max(max16(s0)));
            s0 -= delta; t0 = 0.f; t1 = 0.f;
            exp16(s0, p0, t0, t1, orv);
        }
        l += t0 + t1;
#pragma unroll
        for (int ks = 0; ks < 2; ++ks)
#pragma unroll
            for (int d = 0; d < 4; ++d) o[d] = mfma32(lds_b128(kb + 16384 + (ks * 4 + d) * 1024), pfrag(p0, 4 * ks), o[d]);
    };
    WAIT_BAR(0);
    {
        LAS const char* kb = lds + lane * 16;
        f32x16 s0 = f32x16{};
#pragma unroll
        for (int s = 0; s < 8; ++s) s0 = mfma32(lds_b128(kb + s * 1024), qf[s], s0);
        if (tbq == 0) diag_mask(s0, r, h);
        const float nm = -(swap32_max(max16(s0)) + SM_BIAS);
#pragma unroll
        for (int i = 0; i < 16; ++i) negm[i] = nm;
    }
    for (int t = 0; t < NT - 2; t += 2) {
        if (t) WAIT_BAR(0);
        stage(1, t + 1);
        tile64(0);
        WAIT_BAR(0);
        stage(0, t + 2);
        tile64(1);
    }
    if (NT > 2) WAIT_BAR(0);
    stage(1, NT - 1);
    tile32(0, 0, wid == 0);
    if (wid >= 1) tile32(0, 1, wid == 1);
    WAIT_BAR(0);
    if (wid >= 2) tile32(1, 0, wid == 2);
    if (wid >= 3) tile32(1, 1, true);
    const float inv = 1.f / swap32_add(l);
    char* dst = ws + WS_CTX + (size_t)(tbq * 256 + hd * 8) * 1024 + lane * 16;
#pragma unroll
    for (int d = 0; d < 4; ++d) { *(u32x4*)(dst + (2 * d) * 1024) = pack8(o[d], 0, inv); *(u32x4*)(dst + (2 * d + 1) * 1024) = pack8(o[d], 8, inv); }
}

DI void attnA(LAS char* lds, char* ws, int hg, int tg, int wid, int lane) {
    const int hd = 4 * hg + wid, tb0 = 2 * tg;
    const char* kvsrc = ws + ((wid & 2) ? WS_VA : WS_KA) + (wid & 1) * 4096 + lane * 16;
    auto stage = [&](int buf, int t) {
        LAS char* b = lds + buf * 16384 + wid * 4096;
#pragma unroll
        for (int i = 0; i < 4; ++i) glds16(kvsrc + (size_t)t * 8192 + i * 1024, b + i * 1024);
    };
    stage(0, 0);
    bf16x8 qf[2][2];
#pragma unroll
    for (int u = 0; u < 2; ++u)
#pragma unroll
        for (int s = 0; s < 2; ++s) qf[u][s] = *(const bf16x8*)(ws + WS_QA + (size_t)((hd * NTB + tb0 + u) * 2 + s) * 1024 + lane * 16);
    f32x16 o[2], negm[2]; float l[2] = {0.f, 0.f};
    o[0] = f32x16{}; o[1] = f32x16{};
    auto blk32 = [&](LAS const char* kb) {
        const bf16x8 k0 = lds_b128(kb), k1 = lds_b128(kb + 1024), v0 = lds_b128(kb + 8192), v1 = lds_b128(kb + 8192 + 1024);
        f32x16 s0 = mfma32(k0, qf[0][0], negm[0]), s1 = mfma32(k0, qf[1][0], negm[1]);
        s0 = mfma32(k1, qf[0][1], s0); s1 = mfma32(k1, qf[1][1], s1);
        unsigned p0[8], p1[8], orv = 0; float a0 = 0.f, a1 = 0.f, b0 = 0.f, b1 = 0.f;
        exp16(s0, p0, a0, a1, orv); exp16(s1, p1, b0, b1, orv);
        if (__builtin_expect(__any((orv & 0x40004000u) != 0u), 0)) {
            const float d0 = fmaxf(swap32_max(max16(s0)) + SM_BIAS, 0.f), d1 = fmaxf(swap32_max(max16(s1)) + SM_BIAS, 0.f);
            const float al0 = __builtin_amdgcn_exp2f(-d0), al1 = __builtin_amdgcn_exp2f(-d1);
            l[0] *= al0; o[0] *= al0; negm[0] -= d0; s0 -= d0; l[1] *= al1; o[1] *= al1; negm[1] -= d1; s1 -= d1;
            a0 = a1 = b0 = b1 = 0.f;
            exp16(s0, p0, a0, a1, orv); exp16(s1, p1, b0, b1, orv);
        }
        l[0] += a0 + a1; l[1] += b0 + b1;
        o[0] = mfma32(v0, pfrag(p0, 0), o[0]); o[1] = mfma32(v0, pfrag(p1, 0), o[1]);
        o[0] = mfma32(v1, pfrag(p0, 4), o[0]); o[1] = mfma32(v1, pfrag(p1, 4), o[1]);
    };
    WAIT_BAR(0);
    {
        LAS const char* kb = lds + lane * 16;
        const bf16x8 k0 = lds_b128(kb), k1 = lds_b128(kb + 1024);
#pragma unroll
        for (int u = 0; u < 2; ++u) {
            f32x16 s0 = mfma32(k0, qf[u][0], f32x16{}); s0 = mfma32(k1, qf[u][1], s0);
            const float nm = -(swap32_max(max16(s0)) + SM_BIAS);
#pragma unroll
            for (int i = 0; i < 16; ++i) negm[u][i] = nm;
        }
    }
    for (int t = 0; t < 16; t += 2) {
        if (t) WAIT_BAR(0);
        stage(1, t + 1);
#pragma unroll
        for (int j = 0; j < 4; ++j) blk32(lds + j * 2048 + lane * 16);
        WAIT_BAR(0);
        if (t + 2 < 16) stage(0, t + 2);
#pragma unroll
        for (int j = 0; j < 4; ++j) blk32(lds + 16384 + j * 2048 + lane * 16);
    }
#pragma unroll
    for (int u = 0; u < 2; ++u) {
        const float inv = 1.f / swap32_add(l[u]);
        char* dst = ws + WS_OA + (size_t)((tb0 + u) * 64 + hd * 2) * 1024 + lane * 16;
        *(u32x4*)dst = pack8(o[u], 0, inv); *(u32x4*)(dst + 1024) = pack8(o[u], 8, inv);
    }
}

__global__ __launch_bounds__(256, 2) void k_attn(P2 p) {
    extern __shared__ __attribute__((aligned(16))) char smem[];
    LAS char* lds = (LAS char*)smem;
    const int tid = threadIdx.x, wid = __builtin_amdgcn_readfirstlane(tid >> 6), lane = tid & 63;
    const int bid = blockIdx.x;
    if (bid < 256) attnA(lds, p.ws, bid & 7, bid >> 3, wid, lane);
    else { const int i = bid - 256; attnB(lds, p.ws, i & 31, 15 - (i >> 5), wid, lane); }
}

struct P3 { const u16 *woA, *woB; char* ws; u16* out; };

DI void out_loop(LAS char* lds, const char* Af, int S, int tb0, const char* W, int kt0, int nt, f32x16 (&acc)[2][2], int wid, int lane) {
    constexpr int NS = 4, STG = 32768;
    const int kg = wid >> 2, wm = (wid >> 1) & 1, wn = wid & 1, h = lane >> 5;
    const char* asrc = Af + (size_t)((tb0 + (wid >> 1)) * S + 4 * kt0 + (wid & 1) * 2) * 1024 + lane * 16;
    unsigned voW[2];
#pragma unroll
    for (int i = 0; i < 2; ++i) { const int pc = 2 * wid + i, g = pc >> 3, k = (pc & 7) * 8 + (lane >> 3), slot = lane & 7;
        const int col = 64 * g + 32 * ((slot >> 2) ^ ((k >> 1) & 1)) + 8 * (slot & 3); voW[i] = (unsigned)k * 2048u + col * 2; }
    const char* wsrc = W + (size_t)kt0 * 64 * 2048;
    auto stage = [&](int slot, int t) {
        LAS char* base = lds + slot * STG;
        glds16(asrc + (size_t)t * 4096, base + (2 * wid) * 1024);
        glds16(asrc + (size_t)t * 4096 + 1024, base + (2 * wid + 1) * 1024);
        glds16(wsrc + (size_t)t * (64 * 2048) + voW[0], base + 16384 + (2 * wid) * 1024);
        glds16(wsrc + (size_t)t * (64 * 2048) + voW[1], base + 16384 + (2 * wid + 1) * 1024);
    };
    const int blk = (lane >> 4) & 1, q = (lane & 15) >> 2, p = lane & 3, pp = ((p & 1) << 1) | (p >> 1);
    int boff[2];
#pragma unroll
    for (int c = 0; c < 2; ++c) boff[c] = 16384 + wn * 8192 + (32 * kg + 4 * h + q) * 128 + 64 * (c ^ (q >> 1)) + 32 * blk + 8 * pp;
    const int aoff = (2 * wm * 4 + 2 * kg) * 1024 + lane * 16;
#pragma unroll
    for (int t = 0; t < NS - 1; ++t) stage(t, t);
    for (int t = 0; t < nt; ++t) {
        if (t + 2 < nt) { WAIT_BAR(8); } else if (t + 1 < nt) { WAIT_BAR(4); } else { WAIT_BAR(0); }
        if (t + NS - 1 < nt) stage((t + NS - 1) & 3, t + NS - 1);
        LAS const char* sa = lds + (t & 3) * STG;
#pragma unroll
        for (int ss = 0; ss < 2; ++ss) {
            bf16x8 af[2], wf[2];
#pragma unroll
            for (int c = 0; c < 2; ++c) wf[c] = cat8(lds_tr(sa + boff[c] + ss * 2048), lds_tr(sa + boff[c] + ss * 2048 + 1024));
#pragma unroll
            for (int m = 0; m < 2; ++m) af[m] = lds_b128(sa + aoff + (m * 4 + ss) * 1024);
#pragma unroll
            for (int m = 0; m < 2; ++m)
#pragma unroll
                for (int c = 0; c < 2; ++c) acc[m][c] = mfma32(wf[c], af[m], acc[m][c]);
        }
    }
    WAIT_BAR(0);
}
DI void out_reduce(LAS char* lds, f32x16 (&acc)[2][2], f32x16 (&res)[2], int wid, int lane) {
    const int kg = wid >> 2;
    LAS char* mine = lds + wid * 8192 + lane * 16; LAS const char* other = lds + (wid ^ 4) * 8192 + lane * 16;
#pragma unroll
    for (int c = 0; c < 2; ++c)
#pragma unroll
        for (int i = 0; i < 4; ++i) { f32x4 v;
#pragma unroll
            for (int j = 0; j < 4; ++j) v[j] = kg ? acc[0][c][4 * i + j] : acc[1][c][4 * i + j];
            *(LAS f32x4*)(mine + (c * 4 + i) * 1024) = v; }
    WAIT_BAR(0);
#pragma unroll
    for (int c = 0; c < 2; ++c)
#pragma unroll
        for (int i = 0; i < 4; ++i) { const f32x4 v = *(LAS const f32x4*)(other + (c * 4 + i) * 1024);
#pragma unroll
            for (int j = 0; j < 4; ++j) res[c][4 * i + j] = (kg ? acc[1][c][4 * i + j] : acc[0][c][4 * i + j]) + v[j]; }
    WAIT_BAR(0);
}
DI void out_store(u16* out, int tb0, int col0, const f32x16 (&res)[2], int wid, int lane) {
    const int kg = wid >> 2, wm = (wid >> 1) & 1, wn = wid & 1, r = lane & 31, h = lane >> 5;
    char* dst = (char*)out + (size_t)((tb0 + 2 * wm + kg) * 32 + r) * 4096 + (col0 + 64 * wn + 8 * h) * 2;
#pragma unroll
    for (int c = 0; c < 2; ++c) { *(u32x4*)(dst + c * 64) = pack8(res[c], 0, 1.f); *(u32x4*)(dst + c * 64 + 32) = pack8(res[c], 8, 1.f); }
}

__global__ __launch_bounds__(512, 2) void k_out(P3 p) {
    extern __shared__ __attribute__((aligned(16))) char smem[];
    LAS char* lds = (LAS char*)smem;
    const int tid = threadIdx.x, wid = __builtin_amdgcn_readfirstlane(tid >> 6), lane = tid & 63;
    const int bid = blockIdx.x, role = ((bid >> 3) & 1) ^ 1, tau = (bid & 7) + 8 * (bid >> 4);
    const int pn = tau & 7, pm = tau >> 3, tb0 = pm * 4;
    char* part = p.ws + WS_PART + (size_t)tau * 65536 + wid * 8192 + lane * 16;
    GAS unsigned* flag = (GAS unsigned*)(p.ws + WS_FLAG) + tau * 16;
    f32x16 acc[2][2], res[2];
#pragma unroll
    for (int m = 0; m < 2; ++m) { acc[m][0] = f32x16{}; acc[m][1] = f32x16{}; }
    if (role == 1) {
        out_loop(lds, p.ws + WS_CTX, 256, tb0, (const char*)p.woB + pn * 256, 40, 24, acc, wid, lane);
        out_reduce(lds, acc, res, wid, lane);
        const __amdgpu_buffer_rsrc_t rs = __builtin_amdgcn_make_buffer_rsrc(p.ws + WS_PART + (size_t)tau * 65536, 0, 65536, 0x00020000);
#pragma unroll
        for (int c = 0; c < 2; ++c)
#pragma unroll
            for (int i = 0; i < 4; ++i) { u32x4 v;
#pragma unroll
                for (int j = 0; j < 4; ++j) v[j] = __float_as_uint(res[c][4 * i + j]);
                __builtin_amdgcn_raw_buffer_store_b128(v, rs, wid * 8192 + (c * 4 + i) * 1024 + lane * 16, 0, 16); }
        WAIT_BAR(0);
        if (tid == 0) __hip_atomic_store(flag, 1u, __ATOMIC_RELAXED, __HIP_MEMORY_SCOPE_AGENT);
#pragma unroll
        for (int m = 0; m < 2; ++m) { acc[m][0] = f32x16{}; acc[m][1] = f32x16{}; }
        out_loop(lds, p.ws + WS_OA, 64, tb0, (const char*)p.woA + pn * 256, 0, 16, acc, wid, lane);
        out_reduce(lds, acc, res, wid, lane);
        out_store(p.out, tb0, pn * 128, res, wid, lane);
    } else {
        out_loop(lds, p.ws + WS_CTX, 256, tb0, (const char*)p.woB + pn * 256, 0, 40, acc, wid, lane);
        out_reduce(lds, acc, res, wid, lane);
        if (wid == 0) {
            while ((unsigned)__builtin_amdgcn_readfirstlane(__hip_atomic_load(flag, __ATOMIC_RELAXED, __HIP_MEMORY_SCOPE_AGENT)) != 1u) __builtin_amdgcn_s_sleep(2);
            __builtin_amdgcn_fence(__ATOMIC_ACQUIRE, "agent");
        }
        WAIT_BAR(0);
#pragma unroll
        for (int c = 0; c < 2; ++c)
#pragma unroll
            for (int i = 0; i < 4; ++i) { const f32x4 v = *(const f32x4*)(part + (c * 4 + i) * 1024);
#pragma unroll
                for (int j = 0; j < 4; ++j) res[c][4 * i + j] += v[j]; }
        out_store(p.out, tb0, 1024 + pn * 128, res, wid, lane);
    }
}

extern "C" void kernel_launch(void* const* d_in, const int* in_sizes, int n_in, void* d_out, int out_size, void* d_ws, size_t ws_size,
                              hipStream_t stream) {
    (void)in_sizes; (void)n_in; (void)out_size; (void)ws_size;
    hipFuncSetAttribute((const void*)k_proj, hipFuncAttributeMaxDynamicSharedMemorySize, 131072);
    hipFuncSetAttribute((const void*)k_attn, hipFuncAttributeMaxDynamicSharedMemorySize, 65536);
    hipFuncSetAttribute((const void*)k_out, hipFuncAttributeMaxDynamicSharedMemorySize, 131072);
    P1 p1; p1.xA = (const u16*)d_in[0]; p1.wqA = (const u16*)d_in[1]; p1.wkA = (const u16*)d_in[2]; p1.wvA = (const u16*)d_in[3];
    p1.xB = (const u16*)d_in[5]; p1.cKV = (const u16*)d_in[6]; p1.wqB = (const u16*)d_in[7]; p1.wukB = (const u16*)d_in[8]; p1.wuvB = (const u16*)d_in[9];
    p1.ws = (char*)d_ws;
    k_proj<<<256, 512, 131072, stream>>>(p1);
    P2 p2; p2.ws = (char*)d_ws;
    k_attn<<<768, 256, 65536, stream>>>(p2);
    P3 p3; p3.woA = (const u16*)d_in[4]; p3.woB = (const u16*)d_in[10]; p3.ws = (char*)d_ws; p3.out = (u16*)d_out;
    k_out<<<256, 512, 131072, stream>>>(p3);
}
```

```cpp
#include <hip/hip_runtime.h>
#include <hip/hip_bf16.h>
#include <type_traits>

typedef unsigned short u16;
typedef short bf16x8 __attribute__((ext_vector_type(8)));
typedef short bf16x4 __attribute__((ext_vector_type(4)));
typedef float f32x16 __attribute__((ext_vector_type(16)));
typedef float f32x4 __attribute__((ext_vector_type(4)));
typedef float f32x2 __attribute__((ext_vector_type(2)));
typedef __bf16 bf16x2_t __attribute__((ext_vector_type(2)));
typedef unsigned u32x4 __attribute__((ext_vector_type(4)));
#define LAS __attribute__((address_space(3)))
#define GAS __attribute__((address_space(1)))
#define DI __device__ __forceinline__

DI f32x16 mfma32(bf16x8 a, bf16x8 b, f32x16 c) { return __builtin_amdgcn_mfma_f32_32x32x16_bf16(a, b, c, 0, 0, 0); }
DI unsigned cvtpk(float lo, float hi) { f32x2 v = {lo, hi}; bf16x2_t b = __builtin_convertvector(v, bf16x2_t); return __builtin_bit_cast(unsigned, b); }
DI void glds16(const void* sbase, unsigned voff, LAS void* l) {
    unsigned sv; asm volatile("s_mov_b32 %0, m0\n\ts_mov_b32 m0, %3\n\ts_nop 0\n\tglobal_load_lds_dwordx4 %1, %2\n\ts_mov_b32 m0, %0"
                              : "=&s"(sv) : "v"(voff), "s"(sbase), "s"((unsigned)(size_t)l) : "memory"); }
DI void l2pf(const void* sbase, unsigned voff, LAS void* scratch) {
    unsigned sv; asm volatile("s_mov_b32 %0, m0\n\ts_mov_b32 m0, %3\n\ts_nop 0\n\tglobal_load_lds_dword %1, %2\n\ts_mov_b32 m0, %0"
                              : "=&s"(sv) : "v"(voff), "s"(sbase), "s"((unsigned)(size_t)scratch) : "memory"); }
DI void glds16v(const void* vaddr, LAS void* l) {
    unsigned sv; asm volatile("s_mov_b32 %0, m0\n\ts_mov_b32 m0, %2\n\ts_nop 0\n\tglobal_load_lds_dwordx4 %1, off\n\ts_mov_b32 m0, %0"
                              : "=&s"(sv) : "v"(vaddr), "s"((unsigned)(size_t)l) : "memory"); }
DI void glds16x4(const void* sbase, unsigned voff, LAS void* l) {
    unsigned sv; asm volatile("s_mov_b32 %0, m0\n\ts_mov_b32 m0, %3\n\ts_nop 0\n\tglobal_load_lds_dwordx4 %1, %2\n\tglobal_load_lds_dwordx4 %1, %2 offset:1024\n\t"
                              "global_load_lds_dwordx4 %1, %2 offset:2048\n\tglobal_load_lds_dwordx4 %1, %2 offset:3072\n\ts_mov_b32 m0, %0"
                              : "=&s"(sv) : "v"(voff), "s"(sbase), "s"((unsigned)(size_t)l) : "memory"); }
DI void glds16x2(const void* sbase, unsigned voff, LAS void* l) {
    unsigned sv; asm volatile("s_mov_b32 %0, m0\n\ts_mov_b32 m0, %3\n\ts_nop 0\n\tglobal_load_lds_dwordx4 %1, %2\n\tglobal_load_lds_dwordx4 %1, %2 offset:1024\n\ts_mov_b32 m0, %0"
                              : "=&s"(sv) : "v"(voff), "s"(sbase), "s"((unsigned)(size_t)l) : "memory"); }
DI bf16x8 lds_b128(LAS const char* p) { return *(LAS const bf16x8*)p; }
DI bf16x4 lds_tr(LAS const char* p) { return __builtin_amdgcn_ds_read_tr16_b64_v4i16((LAS bf16x4*)p); }
DI bf16x8 cat8(bf16x4 a, bf16x4 b) { return (bf16x8){a[0], a[1], a[2], a[3], b[0], b[1], b[2], b[3]}; }
DI float swap32_max(float x) { auto rr = __builtin_amdgcn_permlane32_swap(__float_as_uint(x), __float_as_uint(x), false, false); return fmaxf(__uint_as_float(rr[0]), __uint_as_float(rr[1])); }
DI float swap32_add(float x) { auto rr = __builtin_amdgcn_permlane32_swap(__float_as_uint(x), __float_as_uint(x), false, false); return __uint_as_float(rr[0]) + __uint_as_float(rr[1]); }
#define WAIT_BAR(N) asm volatile("s_waitcnt vmcnt(" #N ") lgkmcnt(0)\n\ts_barrier" ::: "memory")
#define SBAR() __builtin_amdgcn_sched_barrier(0)
#define PIN(x) asm volatile("" : "+v"(x))
#define WAIT_BAR_I(N) asm volatile("s_waitcnt vmcnt(%0) lgkmcnt(0)\n\ts_barrier" :: "i"(N) : "memory")

DI u32x4 pack8(const f32x16& a, int b, float sc) {
    u32x4 w; w.x = cvtpk(a[b + 0] * sc, a[b + 1] * sc); w.y = cvtpk(a[b + 2] * sc, a[b + 3] * sc);
    w.z = cvtpk(a[b + 4] * sc, a[b + 5] * sc); w.w = cvtpk(a[b + 6] * sc, a[b + 7] * sc); return w;
}

constexpr int SEQ = 2048, NTB = SEQ / 32;
constexpr float LOG2E = 1.4426950408889634f;
constexpr float CA = 0.17677669529663687f * LOG2E;
constexpr float CB = 0.08838834764831845f * LOG2E;
#define SMB 64.f
constexpr float SM_BIAS = SMB;

constexpr size_t WS_QB = 0, WS_KB = WS_QB + (16u << 20), WS_VB = WS_KB + (16u << 20), WS_QA = WS_VB + (16u << 20),
                 WS_KA = WS_QA + (4u << 20), WS_VA = WS_KA + (128u << 10), WS_CTX = WS_VA + (128u << 10), WS_OA = WS_CTX + (16u << 20),
                 WS_PART = WS_OA + (4u << 20), WS_FLAG = WS_PART + (8u << 20), WS_END = WS_FLAG + 129 * 64;

#define STAMP(ws, kn, i)
struct P1 { const u16 *xB, *cKV, *wqB, *wukB, *wuvB, *xA, *wkA, *wvA; char* ws; };

template <bool OT>
DI void big_unit(LAS char* lds, const char* Ab, unsigned lda_b, const char* Wb, unsigned ldw_b, int ns, f32x16 (&acc)[4][2], int wid, int lane) {
    const int wr = wid >> 2, wc = wid & 3, r = lane & 31, h = lane >> 5;
    unsigned voA[2], voW[2];
#pragma unroll
    for (int i = 0; i < 2; ++i) {
        const int row = (wid * 2 + i) * 16 + (lane >> 2), ch = (lane & 3) ^ ((row >> 2) & 3);
        voA[i] = (unsigned)row * lda_b + ch * 16;
        const int k = ((wid & 1) * 2 + i) * 8 + (lane >> 3), slot = lane & 7;
        const int col = 64 * (wid >> 1) + 32 * ((slot >> 2) ^ ((k >> 1) & 1)) + 8 * (slot & 3);
        voW[i] = (unsigned)k * ldw_b + col * 2;
    }
    int aoff[2], boff[2];
#pragma unroll
    for (int s = 0; s < 2; ++s) aoff[s] = (128 * wr + r) * 64 + (((2 * s + h) ^ ((r >> 2) & 3)) << 4);
    { const int blk = (lane >> 4) & 1, q = (lane & 15) >> 2, p = lane & 3;
#pragma unroll
      for (int n = 0; n < 2; ++n) boff[n] = 16384 + wc * 4096 + (8 * h + q) * 128 + 64 * (n ^ (q >> 1)) + 32 * blk + 8 * p; }
#pragma unroll
    for (int m = 0; m < 4; ++m) { acc[m][0] = f32x16{}; acc[m][1] = f32x16{}; }
    auto stage1 = [&](int t, int i) {
        LAS char* base = lds + (t & 3) * 32768;
        if (i < 2) glds16(Ab + (size_t)t * 64, voA[i], base + (wid * 2 + i) * 1024);
        else glds16(Wb + (size_t)t * 32 * ldw_b, voW[i - 2], base + 16384 + (wid >> 1) * 4096 + ((wid & 1) * 2 + i - 2) * 1024);
    };
    bf16x8 af[2][4], wf[2][2];
    auto rd = [&](int rs, int t, int s, int i) {
        LAS const char* sa = lds + (t & 3) * 32768;
        if (i < 2) wf[rs][i] = cat8(lds_tr(sa + boff[i] + s * 2048), lds_tr(sa + boff[i] + s * 2048 + 512));
        else af[rs][i - 2] = lds_b128(sa + aoff[s] + (i - 2) * 2048);
    };
    auto kstep = [&](int rs, int nt_, int nstep, int st) {
#pragma unroll
        for (int i = 0; i < 8; ++i) {
            const int m = i >> 1, n = i & 1;
            acc[m][n] = OT ? mfma32(wf[rs][n], af[rs][m], acc[m][n]) : mfma32(af[rs][m], wf[rs][n], acc[m][n]);
            if (i < 6) rd(rs ^ 1, nt_, nstep, i);
            if (rs == 1 && (i & 1) && st < ns) stage1(st, i >> 1);
            SBAR();
        }
    };
#pragma unroll
    for (int t = 0; t < 4; ++t)
#pragma unroll
        for (int i = 0; i < 4; ++i) stage1(t, i);
    WAIT_BAR(12);
#pragma unroll
    for (int i = 0; i < 6; ++i) rd(0, 0, 0, i);
    for (int t = 0; t < ns; ++t) {
        kstep(0, t, 1, 0);
        if (t + 3 < ns) { WAIT_BAR(8); } else if (t + 2 < ns) { WAIT_BAR(4); } else { WAIT_BAR(0); }
        kstep(1, t + 1, 0, t + 4);
    }
    WAIT_BAR(0);
}

DI void kv_unit(LAS char* lds, const P1& p, int tb, int wid, int lane) {
    const int r = lane & 31, h = lane >> 5;
    LAS char* wreg = lds + wid * 16384;
    { const int slot = lane & 7;
#pragma unroll
      for (int i = 0; i < 16; ++i) { const int k = i * 8 + (lane >> 3), half = (slot >> 2) ^ ((k >> 1) & 1);
          glds16v((const char*)(half ? p.wvA : p.wkA) + (size_t)(128 * wid + k) * 64 + (slot & 3) * 16, wreg + i * 1024); } }
    bf16x8 xf[8];
#pragma unroll
    for (int s = 0; s < 8; ++s) xf[s] = *(const bf16x8*)((const char*)p.xA + (size_t)(32 * tb + r) * 2048 + (128 * wid + 16 * s + 8 * h) * 2);
    asm volatile("s_waitcnt vmcnt(0)" ::: "memory");
    const int blk = (lane >> 4) & 1, q = (lane & 15) >> 2, pq = lane & 3;
    f32x16 ak = f32x16{}, av = f32x16{};
#pragma unroll
    for (int s = 0; s < 8; ++s) {
        LAS const char* b0 = wreg + (16 * s + 8 * h + q) * 128 + 32 * blk + 8 * pq;
        const bf16x8 kf = cat8(lds_tr(b0 + 64 * (q >> 1)), lds_tr(b0 + 64 * (q >> 1) + 512)), vf = cat8(lds_tr(b0 + 64 * (1 ^ (q >> 1))), lds_tr(b0 + 64 * (1 ^ (q >> 1)) + 512));
        ak = mfma32(kf, xf[s], ak); av = mfma32(xf[s], vf, av);
    }
    asm volatile("s_waitcnt lgkmcnt(0)" ::: "memory");
#pragma unroll
    for (int i = 0; i < 4; ++i) { *(LAS f32x4*)(wreg + i * 1024 + lane * 16) = (f32x4){ak[4 * i], ak[4 * i + 1], ak[4 * i + 2], ak[4 * i + 3]};
                                  *(LAS f32x4*)(wreg + 4096 + i * 1024 + lane * 16) = (f32x4){av[4 * i], av[4 * i + 1], av[4 * i + 2], av[4 * i + 3]}; }
    WAIT_BAR(0);
    if (wid < 4) {
        const int kv = wid >> 1, hf = wid & 1; f32x4 s0 = {0.f, 0.f, 0.f, 0.f}, s1 = {0.f, 0.f, 0.f, 0.f};
#pragma unroll
        for (int w = 0; w < 8; ++w) { s0 += *(LAS const f32x4*)(lds + w * 16384 + kv * 4096 + (2 * hf) * 1024 + lane * 16); s1 += *(LAS const f32x4*)(lds + w * 16384 + kv * 4096 + (2 * hf + 1) * 1024 + lane * 16); }
        u32x4 o; o.x = cvtpk(s0[0], s0[1]); o.y = cvtpk(s0[2], s0[3]); o.z = cvtpk(s1[0], s1[1]); o.w = cvtpk(s1[2], s1[3]);
        *(u32x4*)(p.ws + (kv ? WS_VA : WS_KA) + (size_t)(tb * 2 + hf) * 1024 + lane * 16) = o;
    }
    WAIT_BAR(0);
}

__global__ __launch_bounds__(512, 2) void k_proj(P1 p) {
    extern __shared__ __attribute__((aligned(16))) char smem[];
    LAS char* lds = (LAS char*)smem;
    const int tid = threadIdx.x, wid = __builtin_amdgcn_readfirstlane(tid >> 6), lane = tid & 63;
    const int bid = blockIdx.x, xcd = bid & 7, idx = bid >> 3;
    STAMP(p.ws, 0, 0);
    if (bid == 0 && tid < 128) ((unsigned*)(p.ws + WS_FLAG))[tid * 16] = 0u;
    const int wr = wid >> 2, wc = wid & 3;
    const int pm = idx & 7, pn = 2 * xcd + ((idx >> 3) & 1), kind = idx >> 4;
    f32x16 acc[4][2];
    auto store_f = [&](char* base, float sc) {
#pragma unroll
        for (int m = 0; m < 4; ++m)
#pragma unroll
            for (int n = 0; n < 2; ++n) { const int tb = pm * 8 + wr * 4 + m, fb = pn * 8 + wc * 2 + n, hd = fb >> 2, db = fb & 3;
                char* dst = base + (size_t)((hd * NTB + tb) * 8 + 2 * db) * 1024 + lane * 16;
                *(u32x4*)dst = pack8(acc[m][n], 0, sc); *(u32x4*)(dst + 1024) = pack8(acc[m][n], 8, sc); }
    };
    if (kind == 0) {
        if ((idx & 8) == 0) kv_unit(lds, p, xcd * 8 + pm, wid, lane);
        big_unit<true>(lds, (const char*)p.xB + (size_t)pm * 256 * 2048, 2048, (const char*)p.wqB + pn * 512, 8192, 32, acc, wid, lane);
        store_f(p.ws + WS_QB, CB);
    } else {
        big_unit<true>(lds, (const char*)p.cKV + (size_t)pm * 256 * 1024, 1024, (const char*)p.wukB + pn * 512, 8192, 16, acc, wid, lane);
        store_f(p.ws + WS_KB, 1.f);
        big_unit<false>(lds, (const char*)p.cKV + (size_t)pm * 256 * 1024, 1024, (const char*)p.wuvB + pn * 512, 8192, 16, acc, wid, lane);
#pragma unroll
        for (int m = 0; m < 4; ++m)
#pragma unroll
            for (int n = 0; n < 2; ++n) { const int tb = pm * 8 + wr * 4 + m, fb = pn * 8 + wc * 2 + n, hd = fb >> 2, db = fb & 3;
                char* dst = p.ws + WS_VB + (size_t)((hd * NTB + tb) * 8 + db) * 1024 + lane * 16;
                *(u32x4*)dst = pack8(acc[m][n], 0, 1.f); *(u32x4*)(dst + 4096) = pack8(acc[m][n], 8, 1.f); }
    }
    STAMP(p.ws, 0, 1); STAMP(p.ws, 0, 2); STAMP(p.ws, 0, 3);
}

struct P2 { const u16 *xA, *wqA; char* ws; };

DI void exp16(const f32x16& s, unsigned (&pk)[8], float& t0, float& t1, unsigned& orv) {
#pragma unroll
    for (int i = 0; i < 8; ++i) {
        const float a = __builtin_amdgcn_exp2f(s[2 * i]), b = __builtin_amdgcn_exp2f(s[2 * i + 1]);
        t0 += a; t1 += b; pk[i] = cvtpk(a, b); orv |= pk[i];
    }
}
DI float max16(const f32x16& s) {
    float m = fmaxf(s[0], s[1]);
#pragma unroll
    for (int i = 2; i < 16; ++i) m = fmaxf(m, s[i]);
    return m;
}
DI void diag_mask(f32x16& s, int r, int h) {
#pragma unroll
    for (int i = 0; i < 16; ++i) { const int kr = (i & 3) + 8 * (i >> 2) + 4 * h; if (kr > r) s[i] = -__builtin_inff(); }
}
DI bf16x8 pfrag(const unsigned (&pk)[8], int b) { u32x4 w = {pk[b], pk[b + 1], pk[b + 2], pk[b + 3]}; return __builtin_bit_cast(bf16x8, w); }

DI void attnB(LAS char* lds, char* ws, int hd, int xb, int wid, int lane) {
    const int r = lane & 31, h = lane >> 5;
    const int tbq = 4 * xb + wid, NT = 2 * (xb + 1);
    const char* kbase = ws + WS_KB + (size_t)hd * NTB * 8192 + wid * 4096;
    const char* vbase = ws + WS_VB + (size_t)hd * NTB * 8192 + wid * 4096;
    auto stageK1 = [&](int t, int i) { glds16(kbase + (size_t)t * 16384 + i * 1024, lane * 16, lds + (t & 1) * 16384 + wid * 4096 + i * 1024); };
    auto stageV1 = [&](int t, int i) { glds16(vbase + (size_t)t * 16384 + i * 1024, lane * 16, lds + 32768 + (t & 1) * 16384 + wid * 4096 + i * 1024); };
    glds16x4(kbase, lane * 16, lds + wid * 4096); glds16x4(vbase, lane * 16, lds + 32768 + wid * 4096); glds16x4(kbase + 16384, lane * 16, lds + 16384 + wid * 4096);
    bf16x8 qf[8];
#pragma unroll
    for (int s = 0; s < 8; ++s) qf[s] = *(const bf16x8*)(ws + WS_QB + (size_t)((hd * NTB + tbq) * 8 + s) * 1024 + lane * 16);
    f32x16 o[4], negm, S0, S1; float l = 0.f, t0, t1;
    bf16x8 fr[8]; unsigned pk[8], pn[4];
#pragma unroll
    for (int d = 0; d < 4; ++d) o[d] = f32x16{};
    LAS const char* lbase = lds + lane * 16;
    auto kaddr = [&](int j) { return lbase + ((j >> 1) & 1) * 16384 + (j & 1) * 8192; };
    auto vaddr = [&](int j) { return lbase + 32768 + ((j >> 1) & 1) * 16384 + (j & 1) * 8192; };
    auto slotA = [&](f32x16& Sc, f32x16& Sn, int j, int st) {
        LAS const char* va = vaddr(j);
#pragma unroll
        for (int i = 0; i < 4; ++i) pk[i] = pn[i];
#pragma unroll
        for (int i = 0; i < 8; ++i) {
            Sn = mfma32(fr[i], qf[i], i ? Sn : negm);
            fr[i] = lds_b128(va + i * 1024);
            if (i & 1) { const int e = 8 + (i & 6); const float a = __builtin_amdgcn_exp2f(Sc[e]), b = __builtin_amdgcn_exp2f(Sc[e + 1]); pk[e >> 1] = cvtpk(a, b); t0 += a; t1 += b; PIN(t0); PIN(t1); }
            else if (st >= 0) stageK1(st, i >> 1);
            SBAR();
        }
        const unsigned orv = (pk[0] | pk[1] | pk[2]) | (pk[3] | pk[4] | pk[5]) | (pk[6] | pk[7]);
        if (__builtin_expect(__any((orv & 0x40004000u) != 0u), 0)) {
            const float delta = fmaxf(swap32_max(max16(Sc)) + SM_BIAS, 0.f), alpha = __builtin_amdgcn_exp2f(-delta);
            l *= alpha;
#pragma unroll
            for (int d = 0; d < 4; ++d) o[d] *= alpha;
            negm -= delta; Sn -= delta; t0 = 0.f; t1 = 0.f;
#pragma unroll
            for (int i = 0; i < 8; ++i) { const float a = __builtin_amdgcn_exp2f(Sc[2 * i] - delta), b = __builtin_amdgcn_exp2f(Sc[2 * i + 1] - delta); pk[i] = cvtpk(a, b); t0 += a; t1 += b; }
        }
        l += t0 + t1;
        if (j + 1 == tbq) diag_mask(Sn, r, h);
    };
    auto slotB = [&](f32x16& Sn, int j, int st) {
        LAS const char* ka = kaddr(j + 2);
        t0 = 0.f; t1 = 0.f;
#pragma unroll
        for (int i = 0; i < 8; ++i) {
            o[i & 3] = mfma32(fr[i], pfrag(pk, 4 * (i >> 2)), o[i & 3]);
            fr[i] = lds_b128(ka + i * 1024);
            if (i & 1) { const int e = i & 6; const float a = __builtin_amdgcn_exp2f(Sn[e]), b = __builtin_amdgcn_exp2f(Sn[e + 1]); pn[e >> 1] = cvtpk(a, b); t0 += a; t1 += b; PIN(pn[e >> 1]); PIN(t0); PIN(t1); }
            else if (st >= 0) stageV1(st, i >> 1);
            SBAR();
        }
    };
    WAIT_BAR(0);
    {
        S0 = f32x16{};
#pragma unroll
        for (int s = 0; s < 8; ++s) fr[s] = lds_b128(lbase + s * 1024);
#pragma unroll
        for (int s = 0; s < 8; ++s) S0 = mfma32(fr[s], qf[s], S0);
#pragma unroll
        for (int s = 0; s < 8; ++s) fr[s] = lds_b128(lbase + 8192 + s * 1024);
        if (tbq == 0) diag_mask(S0, r, h);
        const float nm = -(swap32_max(max16(S0)) + SM_BIAS);
#pragma unroll
        for (int i = 0; i < 16; ++i) negm[i] = nm;
        S0 += nm;
        t0 = 0.f; t1 = 0.f;
#pragma unroll
        for (int i = 0; i < 4; ++i) { const float a = __builtin_amdgcn_exp2f(S0[2 * i]), b = __builtin_amdgcn_exp2f(S0[2 * i + 1]); pn[i] = cvtpk(a, b); t0 += a; t1 += b; }
    }
    for (int j = 0;; j += 2) {
        const int t = j >> 1;
        WAIT_BAR(0);
#define BPRIO_MASK 0
        if ((t & BPRIO_MASK) == 0) __builtin_amdgcn_s_setprio(2); else __builtin_amdgcn_s_setprio(0);
        slotA(S0, S1, j, t + 2 < NT ? t + 2 : -1);  slotB(S1, j, t + 1 < NT ? t + 1 : -1);
        if (j == tbq) break;
        slotA(S1, S0, j + 1, -1); slotB(S0, j + 1, -1);
        if (j + 1 == tbq) break;
    }
    if (wid < 2) WAIT_BAR(0);
    const float inv = 1.f / swap32_add(l);
    char* dst = ws + WS_CTX + (size_t)(tbq * 256 + hd * 8) * 1024 + lane * 16;
#pragma unroll
    for (int d = 0; d < 4; ++d) { *(u32x4*)(dst + (2 * d) * 1024) = pack8(o[d], 0, inv); *(u32x4*)(dst + (2 * d + 1) * 1024) = pack8(o[d], 8, inv); }
}

DI void projA(LAS char* lds, const P2& p, int hg, int tg, bf16x8 (&qf)[2][2], int wid, int lane) {
    constexpr int STG = 24576, NST = 16;
    const int r = lane & 31, h = lane >> 5;
    const char* xsrc = (const char*)p.xA + (size_t)tg * 64 * 2048;
    unsigned voX[2], voW[4];
#pragma unroll
    for (int i = 0; i < 2; ++i) { const int row = (wid * 2 + i) * 8 + (lane >> 3), ch = (lane & 7) ^ ((row >> 1) & 7); voX[i] = (unsigned)row * 2048u + ch * 16; }
#pragma unroll
    for (int i = 0; i < 4; ++i) { const int k = ((wid & 1) * 4 + i) * 8 + (lane >> 3), slot = lane & 7;
        voW[i] = (unsigned)k * 2048u + (128 * hg + 64 * (wid >> 1) + 32 * ((slot >> 2) ^ ((k >> 1) & 1)) + 8 * (slot & 3)) * 2; }
    auto stage1 = [&](int slot, int t, int i) {
        LAS char* base = lds + slot * STG;
        if (i < 2) glds16(xsrc + (size_t)t * 128, voX[i], base + (wid * 2 + i) * 1024);
        else glds16((const char*)p.wqA + (size_t)t * 64 * 2048, voW[i - 2], base + 8192 + (wid >> 1) * 8192 + ((wid & 1) * 4 + i - 2) * 1024);
    };
    int aoff[4];
#pragma unroll
    for (int s = 0; s < 4; ++s) aoff[s] = r * 128 + (((2 * s + h) ^ ((r >> 1) & 7)) << 4);
    const int blk = (lane >> 4) & 1, q = (lane & 15) >> 2, pq = lane & 3;
    const int boff = 8192 + (wid >> 1) * 8192 + (8 * h + q) * 128 + 64 * ((wid & 1) ^ (q >> 1)) + 32 * blk + 8 * pq;
    f32x16 acc[2]; acc[0] = f32x16{}; acc[1] = f32x16{};
    bf16x8 wf[4], a0[4], a1[4];
    auto rd = [&](int slot, int s) { LAS const char* sa = lds + slot * STG;
        wf[s] = cat8(lds_tr(sa + boff + s * 2048), lds_tr(sa + boff + s * 2048 + 512)); a0[s] = lds_b128(sa + aoff[s]); a1[s] = lds_b128(sa + aoff[s] + 4096); };
#pragma unroll
    for (int t = 0; t < 3; ++t)
#pragma unroll
        for (int i = 0; i < 6; ++i) stage1(t, t, i);
    WAIT_BAR(12);
    rd(0, 0); rd(0, 1);
    int slot = 0;
    for (int t = 0; t < NST; ++t) {
        int sn = slot + 1; if (sn == 3) sn = 0;
        rd(slot, 2); acc[0] = mfma32(wf[0], a0[0], acc[0]); acc[1] = mfma32(wf[0], a1[0], acc[1]); SBAR();
        rd(slot, 3); acc[0] = mfma32(wf[1], a0[1], acc[0]); acc[1] = mfma32(wf[1], a1[1], acc[1]); SBAR();
        if (t + 2 < NST) { WAIT_BAR(6); } else { WAIT_BAR(0); }
        rd(sn, 0); acc[0] = mfma32(wf[2], a0[2], acc[0]); acc[1] = mfma32(wf[2], a1[2], acc[1]); SBAR();
        if (t + 3 < NST) { stage1(slot, t + 3, 0); stage1(slot, t + 3, 1); stage1(slot, t + 3, 2); }
        rd(sn, 1); acc[0] = mfma32(wf[3], a0[3], acc[0]); acc[1] = mfma32(wf[3], a1[3], acc[1]); SBAR();
        if (t + 3 < NST) { stage1(slot, t + 3, 3); stage1(slot, t + 3, 4); stage1(slot, t + 3, 5); }
        slot = sn;
    }
#pragma unroll
    for (int u = 0; u < 2; ++u)
#pragma unroll
        for (int s = 0; s < 2; ++s) qf[u][s] = __builtin_bit_cast(bf16x8, pack8(acc[u], 8 * s, CA));
    WAIT_BAR(0);
}
DI void attnA(LAS char* lds, const P2& p, int hg, int tg, int wid, int lane) {
    char* ws = p.ws;
    const int hd = 4 * hg + wid, tb0 = 2 * tg;
    bf16x8 qf[2][2];
    __builtin_amdgcn_s_setprio(3);
    projA(lds, p, hg, tg, qf, wid, lane);
    STAMP(ws, 1, 1);
    __builtin_amdgcn_s_setprio(1);
    STAMP(ws, 1, 2);
    const char* kvsrc = ws + ((wid & 2) ? WS_VA : WS_KA) + (wid & 1) * 4096;
    auto stage = [&](int slot, int t) { glds16x4(kvsrc + (size_t)t * 8192, lane * 16, lds + slot * 16384 + wid * 4096); };
    stage(0, 0); stage(1, 1);
    f32x16 o[2], negm[2], Sa[2], Sb[2]; float l[2] = {0.f, 0.f};
    o[0] = f32x16{}; o[1] = f32x16{};
    bf16x8 kf[2], vf[2]; unsigned pp[2][8];
    LAS const char* lbase = lds + lane * 16;
    auto step = [&](f32x16 (&Sc)[2], f32x16 (&Sn)[2], LAS const char* ka, LAS const char* va) {
        unsigned pn[2][8], orv = 0; float t[2][2] = {{0.f, 0.f}, {0.f, 0.f}};
#pragma unroll
        for (int i = 0; i < 8; ++i) {
            const int u = i & 1, s = (i >> 1) & 1;
            if (i < 4) Sn[u] = mfma32(kf[s], qf[u][s], s ? Sn[u] : negm[u]);
            else o[u] = mfma32(vf[s], pfrag(pp[u], 4 * s), o[u]);
            if (i == 3) { kf[0] = lds_b128(ka); kf[1] = lds_b128(ka + 1024); }
            if (i == 7) { vf[0] = lds_b128(va); vf[1] = lds_b128(va + 1024); }
            const int uu = i >> 2, e = (i & 3) * 4;
            const float a = __builtin_amdgcn_exp2f(Sc[uu][e]), b = __builtin_amdgcn_exp2f(Sc[uu][e + 1]), c = __builtin_amdgcn_exp2f(Sc[uu][e + 2]), d = __builtin_amdgcn_exp2f(Sc[uu][e + 3]);
            pn[uu][e / 2] = cvtpk(a, b); pn[uu][e / 2 + 1] = cvtpk(c, d); orv |= pn[uu][e / 2] | pn[uu][e / 2 + 1];
            t[uu][0] += a; t[uu][1] += b; t[uu][0] += c; t[uu][1] += d; PIN(t[uu][0]); PIN(t[uu][1]);
            SBAR();
        }
        if (__builtin_expect(__any((orv & 0x40004000u) != 0u), 0)) {
#pragma unroll
            for (int u = 0; u < 2; ++u) {
                const float dl = fmaxf(swap32_max(max16(Sc[u])) + SM_BIAS, 0.f), al = __builtin_amdgcn_exp2f(-dl);
                l[u] *= al; o[u] *= al; negm[u] -= dl; Sn[u] -= dl; t[u][0] = 0.f; t[u][1] = 0.f;
#pragma unroll
                for (int i = 0; i < 8; ++i) { const float a = __builtin_amdgcn_exp2f(Sc[u][2 * i] - dl), b = __builtin_amdgcn_exp2f(Sc[u][2 * i + 1] - dl); pn[u][i] = cvtpk(a, b); t[u][0] += a; t[u][1] += b; }
            }
        }
#pragma unroll
        for (int u = 0; u < 2; ++u) { l[u] += t[u][0] + t[u][1];
#pragma unroll
            for (int i = 0; i < 8; ++i) pp[u][i] = pn[u][i]; }
    };
    WAIT_BAR(4);
    {
        kf[0] = lds_b128(lbase); kf[1] = lds_b128(lbase + 1024);
#pragma unroll
        for (int u = 0; u < 2; ++u) {
            Sa[u] = mfma32(kf[0], qf[u][0], f32x16{}); Sa[u] = mfma32(kf[1], qf[u][1], Sa[u]);
            const float nm = -(swap32_max(max16(Sa[u])) + SM_BIAS);
#pragma unroll
            for (int i = 0; i < 16; ++i) negm[u][i] = nm;
            Sa[u] += nm;
#pragma unroll
            for (int i = 0; i < 8; ++i) pp[u][i] = 0u;
        }
        kf[0] = lds_b128(lbase + 2048); kf[1] = lds_b128(lbase + 2048 + 1024);
        vf[0] = kf[0]; vf[1] = kf[1];
    }
    int slot = 0;
    for (int t = 0; t < 16; ++t) {
        WAIT_BAR(0);
        int sn = slot + 1; if (sn == 3) sn = 0; int sp = sn + 1; if (sp == 3) sp = 0;
        if (t + 2 < 16) stage(sp, t + 2);
        LAS const char* cur = lbase + slot * 16384; LAS const char* nxt = lbase + sn * 16384;
        step(Sa, Sb, cur + 2 * 2048, cur + 8192);
        step(Sb, Sa, cur + 3 * 2048, cur + 8192 + 2048);
        step(Sa, Sb, nxt, cur + 8192 + 2 * 2048);
        step(Sb, Sa, nxt + 2048, cur + 8192 + 3 * 2048);
        slot = sn;
    }
#pragma unroll
    for (int s = 0; s < 2; ++s)
#pragma unroll
        for (int u = 0; u < 2; ++u) o[u] = mfma32(vf[s], pfrag(pp[u], 4 * s), o[u]);
#pragma unroll
    for (int u = 0; u < 2; ++u) {
        const float inv = 1.f / swap32_add(l[u]);
        char* dst = ws + WS_OA + (size_t)((tb0 + u) * 64 + hd * 2) * 1024 + lane * 16;
        *(u32x4*)dst = pack8(o[u], 0, inv); *(u32x4*)(dst + 1024) = pack8(o[u], 8, inv);
    }
}

__global__ __launch_bounds__(256, 2) void k_attn(P2 p) {
    extern __shared__ __attribute__((aligned(16))) char smem[];
    LAS char* lds = (LAS char*)smem;
    const int tid = threadIdx.x, wid = __builtin_amdgcn_readfirstlane(tid >> 6), lane = tid & 63;
    const int bid = blockIdx.x;
    const int xcd = bid & 7, idx = bid >> 3, kind = (idx ^ (idx >> 5)) & 1, it = (idx >> 1) & 15 | (idx >> 5) << 4;
    STAMP(p.ws, 1, 0);
    if (kind == 0) { attnA(lds, p, xcd, it, wid, lane); }
    else { const int hd = xcd + 8 * (it >> 3), x = it & 7;
        attnB(lds, p.ws, hd, 15 - x, wid, lane);
        WAIT_BAR(0);
        STAMP(p.ws, 1, 1);
        attnB(lds, p.ws, hd, x, wid, lane); STAMP(p.ws, 1, 2); }
    STAMP(p.ws, 1, 3);
}

struct P3 { const u16 *woA, *woB; char* ws; u16* out; };

DI void out_loop(LAS char* lds, const char* Af, int S, int tb0, const char* W, int kt0, int nt, f32x16 (&acc)[2][2], int wid, int lane) {
    constexpr int STG = 32768;
    if (wid >= 4) {
        const int lw = wid - 4;
        const char* asrc = Af + (size_t)((tb0 + lw) * S + 4 * kt0) * 1024;
        unsigned voW[4];
#pragma unroll
        for (int i = 0; i < 4; ++i) { const int pc = 4 * lw + i, g = pc >> 3, k = (pc & 7) * 8 + (lane >> 3), slot = lane & 7;
            const int col = 64 * g + 32 * ((slot >> 2) ^ ((k >> 1) & 1)) + 8 * (slot & 3); voW[i] = (unsigned)k * 2048u + col * 2; }
        const char* wsrc = W + (size_t)kt0 * 64 * 2048;
        auto stage = [&](int t) {
            LAS char* base = lds + (t & 3) * STG;
            glds16x4(asrc + (size_t)t * 4096, lane * 16, base + lw * 4096);
#pragma unroll
            for (int i = 0; i < 4; ++i) glds16(wsrc + (size_t)t * (64 * 2048), voW[i], base + 16384 + (4 * lw + i) * 1024);
        };
#pragma unroll
        for (int t = 0; t < 4; ++t) stage(t);
        WAIT_BAR(24);
        for (int t = 0; t < nt; ++t) {
            if (t + 3 < nt) { WAIT_BAR(16); } else if (t + 2 < nt) { WAIT_BAR(8); } else { WAIT_BAR(0); }
            if (t + 4 < nt) stage(t + 4);
        }
    } else {
        const int wm = wid >> 1, wn = wid & 1, h = lane >> 5;
        const int blk = (lane >> 4) & 1, q = (lane & 15) >> 2, p = lane & 3, pp = ((p & 1) << 1) | (p >> 1);
        int boff[2];
#pragma unroll
        for (int c = 0; c < 2; ++c) boff[c] = 16384 + wn * 8192 + (4 * h + q) * 128 + 64 * (c ^ (q >> 1)) + 32 * blk + 8 * pp;
        const int aoff = (2 * wm * 4) * 1024 + lane * 16;
        bf16x8 af[4][2], wf[4][2];
        auto rd = [&](int slot, int s, int i) {
            LAS const char* sa = lds + slot * STG;
            if (i & 1) af[s][i >> 1] = lds_b128(sa + aoff + ((i >> 1) * 4 + s) * 1024);
            else wf[s][i >> 1] = cat8(lds_tr(sa + boff[i >> 1] + s * 2048), lds_tr(sa + boff[i >> 1] + s * 2048 + 1024));
        };
        auto kstep = [&](int s, int nslot) {
#pragma unroll
            for (int i = 0; i < 4; ++i) {
                const int m = i >> 1, c = i & 1;
                acc[m][c] = mfma32(wf[s][c], af[s][m], acc[m][c]);
                rd(nslot, (s + 2) & 3, i);
                SBAR();
            }
        };
        WAIT_BAR(0);
#pragma unroll
        for (int i = 0; i < 4; ++i) { rd(0, 0, i); rd(0, 1, i); }
        for (int t = 0; t < nt; ++t) {
            kstep(0, t & 3); kstep(1, t & 3);
            asm volatile("s_waitcnt lgkmcnt(0)\n\ts_barrier" ::: "memory");
            kstep(2, (t + 1) & 3); kstep(3, (t + 1) & 3);
        }
    }
    WAIT_BAR(0);
}
DI void out_store(u16* out, int tb0, int col0, const f32x16 (&acc)[2][2], int wid, int lane) {
    const int wm = wid >> 1, wn = wid & 1, r = lane & 31, h = lane >> 5;
#pragma unroll
    for (int m = 0; m < 2; ++m) {
        char* dst = (char*)out + (size_t)((tb0 + 2 * wm + m) * 32 + r) * 4096 + (col0 + 64 * wn + 8 * h) * 2;
#pragma unroll
        for (int c = 0; c < 2; ++c) { *(u32x4*)(dst + c * 64) = pack8(acc[m][c], 0, 1.f); *(u32x4*)(dst + c * 64 + 32) = pack8(acc[m][c], 8, 1.f); }
    }
}

__global__ __launch_bounds__(512, 2) void k_out(P3 p) {
    extern __shared__ __attribute__((aligned(16))) char smem[];
    LAS char* lds = (LAS char*)smem;
    const int tid = threadIdx.x, wid = __builtin_amdgcn_readfirstlane(tid >> 6), lane = tid & 63;
    const int bid = blockIdx.x, role = ((bid >> 3) & 1) ^ 1, tau = (bid & 7) + 8 * (bid >> 4);
    const int xg = bid & 7, ti = bid >> 4, pm = 4 * (xg >> 1) + (ti >> 2), pn = 4 * (xg & 1) + (ti & 3), tb0 = pm * 4;
    GAS unsigned* flag = (GAS unsigned*)(p.ws + WS_FLAG) + tau * 16;
    f32x16 acc[2][2];
    STAMP(p.ws, 2, 0);
#pragma unroll
    for (int m = 0; m < 2; ++m) { acc[m][0] = f32x16{}; acc[m][1] = f32x16{}; }
    if (role == 1) {
        out_loop(lds, p.ws + WS_CTX, 256, tb0, (const char*)p.woB + pn * 256, 40, 24, acc, wid, lane);
        if (wid < 4) {
            const __amdgpu_buffer_rsrc_t rs = __builtin_amdgcn_make_buffer_rsrc(p.ws + WS_PART + (size_t)tau * 65536, 0, 65536, 0x00020000);
#pragma unroll
            for (int m = 0; m < 2; ++m)
#pragma unroll
                for (int c = 0; c < 2; ++c)
#pragma unroll
                    for (int i = 0; i < 4; ++i) { u32x4 v;
#pragma unroll
                        for (int j = 0; j < 4; ++j) v[j] = __float_as_uint(acc[m][c][4 * i + j]);
                        __builtin_amdgcn_raw_buffer_store_b128(v, rs, wid * 16384 + ((m * 2 + c) * 4 + i) * 1024 + lane * 16, 0, 16); }
        }
        WAIT_BAR(0);
        if (tid == 0) __hip_atomic_store(flag, 1u, __ATOMIC_RELAXED, __HIP_MEMORY_SCOPE_AGENT);
        STAMP(p.ws, 2, 1);
#pragma unroll
        for (int m = 0; m < 2; ++m) { acc[m][0] = f32x16{}; acc[m][1] = f32x16{}; }
        out_loop(lds, p.ws + WS_OA, 64, tb0, (const char*)p.woA + pn * 256, 0, 16, acc, wid, lane);
        STAMP(p.ws, 2, 2);
        if (wid < 4) out_store(p.out, tb0, pn * 128, acc, wid, lane);
        STAMP(p.ws, 2, 3);
    } else {
        out_loop(lds, p.ws + WS_CTX, 256, tb0, (const char*)p.woB + pn * 256, 0, 40, acc, wid, lane);
        STAMP(p.ws, 2, 1);
        if (wid == 0) {
            while ((unsigned)__builtin_amdgcn_readfirstlane(__hip_atomic_load(flag, __ATOMIC_RELAXED, __HIP_MEMORY_SCOPE_AGENT)) != 1u) __builtin_amdgcn_s_sleep(2);
            __builtin_amdgcn_fence(__ATOMIC_ACQUIRE, "agent");
        }
        WAIT_BAR(0);
        if (wid < 4) {
            const char* part = p.ws + WS_PART + (size_t)tau * 65536 + wid * 16384 + lane * 16;
#pragma unroll
            for (int m = 0; m < 2; ++m)
#pragma unroll
                for (int c = 0; c < 2; ++c)
#pragma unroll
                    for (int i = 0; i < 4; ++i) { const f32x4 v = *(const f32x4*)(part + ((m * 2 + c) * 4 + i) * 1024);
#pragma unroll
                        for (int j = 0; j < 4; ++j) acc[m][c][4 * i + j] += v[j]; }
            STAMP(p.ws, 2, 2);
            out_store(p.out, tb0, 1024 + pn * 128, acc, wid, lane);
        }
        STAMP(p.ws, 2, 3);
    }
}

#define BENCH_MARK(s)
extern "C" void kernel_launch(void* const* d_in, const int* in_sizes, int n_in, void* d_out, int out_size, void* d_ws, size_t ws_size,
                              hipStream_t stream) {
    (void)in_sizes; (void)n_in; (void)out_size; (void)ws_size;
    (void)hipFuncSetAttribute((const void*)k_proj, hipFuncAttributeMaxDynamicSharedMemorySize, 131072);
    (void)hipFuncSetAttribute((const void*)k_attn, hipFuncAttributeMaxDynamicSharedMemorySize, 73728);
    (void)hipFuncSetAttribute((const void*)k_out, hipFuncAttributeMaxDynamicSharedMemorySize, 131072);
    P1 p1; p1.xA = (const u16*)d_in[0]; p1.wkA = (const u16*)d_in[2]; p1.wvA = (const u16*)d_in[3];
    p1.xB = (const u16*)d_in[5]; p1.cKV = (const u16*)d_in[6]; p1.wqB = (const u16*)d_in[7]; p1.wukB = (const u16*)d_in[8]; p1.wuvB = (const u16*)d_in[9];
    p1.ws = (char*)d_ws;
    BENCH_MARK(stream);
    k_proj<<<256, 512, 131072, stream>>>(p1);
    BENCH_MARK(stream);
    P2 p2; p2.xA = (const u16*)d_in[0]; p2.wqA = (const u16*)d_in[1]; p2.ws = (char*)d_ws;
    k_attn<<<512, 256, 73728, stream>>>(p2);
    BENCH_MARK(stream);
    P3 p3; p3.woA = (const u16*)d_in[4]; p3.woB = (const u16*)d_in[10]; p3.ws = (char*)d_ws; p3.out = (u16*)d_out;
    k_out<<<256, 512, 131072, stream>>>(p3);
    BENCH_MARK(stream);
}
```

```cpp
#include <hip/hip_runtime.h>
#include <hip/hip_bf16.h>
#include <type_traits>

typedef unsigned short u16;
typedef short bf16x8 __attribute__((ext_vector_type(8)));
typedef short bf16x4 __attribute__((ext_vector_type(4)));
typedef float f32x16 __attribute__((ext_vector_type(16)));
typedef float f32x4 __attribute__((ext_vector_type(4)));
typedef float f32x2 __attribute__((ext_vector_type(2)));
typedef __bf16 bf16x2_t __attribute__((ext_vector_type(2)));
typedef unsigned u32x4 __attribute__((ext_vector_type(4)));
#define LAS __attribute__((address_space(3)))
#define GAS __attribute__((address_space(1)))
#define DI __device__ __forceinline__

DI f32x16 mfma32(bf16x8 a, bf16x8 b, f32x16 c) { return __builtin_amdgcn_mfma_f32_32x32x16_bf16(a, b, c, 0, 0, 0); }
DI unsigned cvtpk(float lo, float hi) { f32x2 v = {lo, hi}; bf16x2_t b = __builtin_convertvector(v, bf16x2_t); return __builtin_bit_cast(unsigned, b); }
DI void glds16(const void* sbase, unsigned voff, LAS void* l) {
    unsigned sv; asm volatile("s_mov_b32 %0, m0\n\ts_mov_b32 m0, %3\n\ts_nop 0\n\tglobal_load_lds_dwordx4 %1, %2\n\ts_mov_b32 m0, %0"
                              : "=&s"(sv) : "v"(voff), "s"(sbase), "s"((unsigned)(size_t)l) : "memory"); }
template <int OFF> DI void glds16o(const void* sbase, unsigned voff, unsigned m0v) {
    asm volatile("s_mov_b32 m0, %2\n\ts_nop 0\n\tglobal_load_lds_dwordx4 %0, %1 offset:%3" :: "v"(voff), "s"(sbase), "s"(m0v), "i"(OFF) : "memory"); }
DI void l2pf(const void* sbase, unsigned voff, LAS void* scratch) {
    unsigned sv; asm volatile("s_mov_b32 %0, m0\n\ts_mov_b32 m0, %3\n\ts_nop 0\n\tglobal_load_lds_dword %1, %2\n\ts_mov_b32 m0, %0"
                              : "=&s"(sv) : "v"(voff), "s"(sbase), "s"((unsigned)(size_t)scratch) : "memory"); }
DI void glds16v(const void* vaddr, LAS void* l) {
    unsigned sv; asm volatile("s_mov_b32 %0, m0\n\ts_mov_b32 m0, %2\n\ts_nop 0\n\tglobal_load_lds_dwordx4 %1, off\n\ts_mov_b32 m0, %0"
                              : "=&s"(sv) : "v"(vaddr), "s"((unsigned)(size_t)l) : "memory"); }
DI void glds16x4(const void* sbase, unsigned voff, LAS void* l) {
    unsigned sv; asm volatile("s_mov_b32 %0, m0\n\ts_mov_b32 m0, %3\n\ts_nop 0\n\tglobal_load_lds_dwordx4 %1, %2\n\tglobal_load_lds_dwordx4 %1, %2 offset:1024\n\t"
                              "global_load_lds_dwordx4 %1, %2 offset:2048\n\tglobal_load_lds_dwordx4 %1, %2 offset:3072\n\ts_mov_b32 m0, %0"
                              : "=&s"(sv) : "v"(voff), "s"(sbase), "s"((unsigned)(size_t)l) : "memory"); }
DI void glds16x2(const void* sbase, unsigned voff, LAS void* l) {
    unsigned sv; asm volatile("s_mov_b32 %0, m0\n\ts_mov_b32 m0, %3\n\ts_nop 0\n\tglobal_load_lds_dwordx4 %1, %2\n\tglobal_load_lds_dwordx4 %1, %2 offset:1024\n\ts_mov_b32 m0, %0"
                              : "=&s"(sv) : "v"(voff), "s"(sbase), "s"((unsigned)(size_t)l) : "memory"); }
DI bf16x8 lds_b128(LAS const char* p) { return *(LAS const bf16x8*)p; }
DI bf16x4 lds_tr(LAS const char* p) { return __builtin_amdgcn_ds_read_tr16_b64_v4i16((LAS bf16x4*)p); }
DI bf16x8 cat8(bf16x4 a, bf16x4 b) { return (bf16x8){a[0], a[1], a[2], a[3], b[0], b[1], b[2], b[3]}; }
DI float swap32_max(float x) { auto rr = __builtin_amdgcn_permlane32_swap(__float_as_uint(x), __float_as_uint(x), false, false); return fmaxf(__uint_as_float(rr[0]), __uint_as_float(rr[1])); }
DI float swap32_add(float x) { auto rr = __builtin_amdgcn_permlane32_swap(__float_as_uint(x), __float_as_uint(x), false, false); return __uint_as_float(rr[0]) + __uint_as_float(rr[1]); }
#define WAIT_BAR(N) asm volatile("s_waitcnt vmcnt(" #N ") lgkmcnt(0)\n\ts_barrier" ::: "memory")
#define SBAR() __builtin_amdgcn_sched_barrier(0)
#define PIN(x) asm volatile("" : "+v"(x))
#define WAIT_BAR_I(N) asm volatile("s_waitcnt vmcnt(%0) lgkmcnt(0)\n\ts_barrier" :: "i"(N) : "memory")

DI void st16(void* p, u32x4 v) { *(u32x4*)p = v; }
DI u32x4 pack8(const f32x16& a, int b, float sc) {
    u32x4 w; w.x = cvtpk(a[b + 0] * sc, a[b + 1] * sc); w.y = cvtpk(a[b + 2] * sc, a[b + 3] * sc);
    w.z = cvtpk(a[b + 4] * sc, a[b + 5] * sc); w.w = cvtpk(a[b + 6] * sc, a[b + 7] * sc); return w;
}

constexpr int SEQ = 2048, NTB = SEQ / 32;
constexpr float LOG2E = 1.4426950408889634f;
constexpr float CA = 0.17677669529663687f * LOG2E;
constexpr float CB = 0.08838834764831845f * LOG2E;
#define SMB 64.f
constexpr float SM_BIAS = SMB;

constexpr size_t WS_QB = 0, WS_KB = WS_QB + (16u << 20), WS_VB = WS_KB + (16u << 20), WS_QA = WS_VB + (16u << 20),
                 WS_KA = WS_QA + (4u << 20), WS_VA = WS_KA + (128u << 10), WS_CTX = WS_VA + (128u << 10), WS_OA = WS_CTX + (16u << 20),
                 WS_PART = WS_OA + (4u << 20), WS_FLAG = WS_PART + (8u << 20), WS_END = WS_FLAG + 129 * 64;

#define STAMP(ws, kn, i)
struct P1 { const u16 *xB, *cKV, *wqB, *wukB, *wuvB, *xA, *wkA, *wvA; char* ws; };

template <bool KV, class Mid>
DI void big_stream(LAS char* lds, const char* Ab, unsigned lda_b, const char* W0, const char* W1, f32x16 (&acc)[4][2], Mid mid, int wid, int lane) {
    constexpr unsigned ldw_b = 8192; constexpr int ns = 32;
    const int wr = wid >> 2, wc = wid & 3, r = lane & 31, h = lane >> 5;
    unsigned voA[2], voW[2];
#pragma unroll
    for (int i = 0; i < 2; ++i) {
        const int row = (wid * 2 + i) * 16 + (lane >> 2), ch = (lane & 3) ^ ((row >> 2) & 3);
        voA[i] = (unsigned)row * lda_b + ch * 16;
        const int k = ((wid & 1) * 2 + i) * 8 + (lane >> 3), slot = lane & 7;
        const int col = 64 * (wid >> 1) + 32 * ((slot >> 2) ^ ((k >> 1) & 1)) + 8 * (slot & 3);
        voW[i] = (unsigned)k * ldw_b + col * 2;
    }
    int aoff[2], boff[2];
#pragma unroll
    for (int s = 0; s < 2; ++s) aoff[s] = (128 * wr + r) * 64 + (((2 * s + h) ^ ((r >> 2) & 3)) << 4);
    { const int blk = (lane >> 4) & 1, q = (lane & 15) >> 2, p = lane & 3;
#pragma unroll
      for (int n = 0; n < 2; ++n) boff[n] = 16384 + wc * 4096 + (8 * h + q) * 128 + 64 * (n ^ (q >> 1)) + 32 * blk + 8 * p; }
#pragma unroll
    for (int m = 0; m < 4; ++m) { acc[m][0] = f32x16{}; acc[m][1] = f32x16{}; }
    auto stage1 = [&](int t, int i) {
        LAS char* base = lds + (t & 3) * 32768;
        const int tk = KV ? (t & 15) : t;
        if (i < 2) glds16(Ab + (size_t)tk * 64, voA[i], base + (wid * 2 + i) * 1024);
        else glds16(((KV && t >= 16) ? W1 : W0) + (size_t)tk * 32 * ldw_b, voW[i - 2], base + 16384 + (wid >> 1) * 4096 + ((wid & 1) * 2 + i - 2) * 1024);
    };
    bf16x8 af[2][4], wf[2][2];
    auto rd = [&](int rs, int t, int s, int i) {
        LAS const char* sa = lds + (t & 3) * 32768;
        if (i < 2) wf[rs][i] = cat8(lds_tr(sa + boff[i] + s * 2048), lds_tr(sa + boff[i] + s * 2048 + 512));
        else af[rs][i - 2] = lds_b128(sa + aoff[s] + (i - 2) * 2048);
    };
    auto kstep = [&](auto OT, int rs, int nt_, int nstep, int st) {
#pragma unroll
        for (int i = 0; i < 8; ++i) {
            const int m = i >> 1, n = i & 1;
            acc[m][n] = OT.value ? mfma32(wf[rs][n], af[rs][m], acc[m][n]) : mfma32(af[rs][m], wf[rs][n], acc[m][n]);
            if (i < 6) rd(rs ^ 1, nt_, nstep, i);
            if (rs == 1 && (i & 1) && st < ns) stage1(st, i >> 1);
            SBAR();
        }
    };
    using TT = std::integral_constant<bool, true>; using FF = std::integral_constant<bool, false>;
#pragma unroll
    for (int t = 0; t < 4; ++t)
#pragma unroll
        for (int i = 0; i < 4; ++i) stage1(t, i);
    WAIT_BAR(12);
#pragma unroll
    for (int i = 0; i < 6; ++i) rd(0, 0, 0, i);
    for (int t = 0; t < (KV ? 16 : 28); ++t) {
        kstep(TT{}, 0, t, 1, 0);
        WAIT_BAR(8);
        if (!KV && t == 18) mid(2);
        kstep(TT{}, 1, t + 1, 0, t + 4);
    }
    if constexpr (!KV) {
        kstep(TT{}, 0, 28, 1, 0); WAIT_BAR(8);  mid(0); kstep(TT{}, 1, 29, 0, ns);
        kstep(TT{}, 0, 29, 1, 0); WAIT_BAR(8);  mid(1); kstep(TT{}, 1, 30, 0, ns);
        kstep(TT{}, 0, 30, 1, 0); WAIT_BAR(16); kstep(TT{}, 1, 31, 0, ns);
        kstep(TT{}, 0, 31, 1, 0); kstep(TT{}, 1, 31, 0, ns);
    } else {
        mid(0);
#pragma unroll
        for (int m = 0; m < 4; ++m) { acc[m][0] = f32x16{}; acc[m][1] = f32x16{}; }
#pragma unroll 1
        for (int t = 16; t < ns; ++t) {
            kstep(FF{}, 0, t, 1, 0);
            if (t < 19) { WAIT_BAR(24); } else if (t + 3 < ns) { WAIT_BAR(8); } else if (t + 2 < ns) { WAIT_BAR(4); } else { WAIT_BAR(0); }
            kstep(FF{}, 1, t + 1, 0, t + 4);
        }
    }
}

struct KvUnit {
    bf16x8 xf[8];
    DI void issue(int part, LAS char* lds, const P1& p, int tb, int isv, int wid, int lane) {
        const char* wsrc = (const char*)(isv ? p.wvA : p.wkA) + wid * 8192;
        if (part == 2) {
            l2pf(wsrc, lane * 128, lds + 131072 + wid * 256);
            l2pf((const char*)p.xA + (size_t)tb * 65536 + wid * 256, (lane >> 1) * 2048 + (lane & 1) * 128, lds + 131072 + wid * 256);
            return;
        }
        glds16x4(wsrc + part * 4096, lane * 16, lds + part * 32768 + wid * 4096);
        if (part) {
#pragma unroll
            for (int s = 0; s < 8; ++s) xf[s] = *(const bf16x8*)((const char*)p.xA + (size_t)(32 * tb + (lane & 31)) * 2048 + (128 * wid + 16 * s + 8 * (lane >> 5)) * 2);
        }
    }
    DI void finish(LAS char* lds, const P1& p, int tb, int isv, int wid, int lane) {
        const int h = lane >> 5, blk = (lane >> 4) & 1, q = (lane & 15) >> 2, pq = lane & 3;
        asm volatile("s_waitcnt vmcnt(16)" ::: "memory");
        f32x16 ak = f32x16{};
#pragma unroll
        for (int s = 0; s < 8; ++s) {
            LAS const char* b0 = lds + (s >> 2) * 32768 + wid * 4096 + (16 * (s & 3) + 8 * h + q) * 64 + 32 * blk + 8 * pq;
            const bf16x8 wf = cat8(lds_tr(b0), lds_tr(b0 + 256));
            if (isv) ak = mfma32(xf[s], wf, ak); else ak = mfma32(wf, xf[s], ak);
        }
        asm volatile("s_waitcnt lgkmcnt(0)" ::: "memory");
#pragma unroll
        for (int i = 0; i < 4; ++i) *(LAS f32x4*)(lds + wid * 4096 + i * 1024 + lane * 16) = (f32x4){ak[4 * i], ak[4 * i + 1], ak[4 * i + 2], ak[4 * i + 3]};
        asm volatile("s_waitcnt lgkmcnt(0)\n\ts_barrier" ::: "memory");
        if (wid < 2) {
            f32x4 s0 = {0.f, 0.f, 0.f, 0.f}, s1 = {0.f, 0.f, 0.f, 0.f};
#pragma unroll
            for (int w = 0; w < 8; ++w) { s0 += *(LAS const f32x4*)(lds + w * 4096 + (2 * wid) * 1024 + lane * 16); s1 += *(LAS const f32x4*)(lds + w * 4096 + (2 * wid + 1) * 1024 + lane * 16); }
            u32x4 o; o.x = cvtpk(s0[0], s0[1]); o.y = cvtpk(s0[2], s0[3]); o.z = cvtpk(s1[0], s1[1]); o.w = cvtpk(s1[2], s1[3]);
            *(u32x4*)(p.ws + (isv ? WS_VA : WS_KA) + (size_t)(tb * 2 + wid) * 1024 + lane * 16) = o;
        }
    }
};

__global__ __launch_bounds__(512, 2) void k_proj(P1 p) {
    extern __shared__ __attribute__((aligned(16))) char smem[];
    LAS char* lds = (LAS char*)smem;
    const int tid = threadIdx.x, wid = __builtin_amdgcn_readfirstlane(tid >> 6), lane = tid & 63;
    const int bid = blockIdx.x, xcd = bid & 7, idx = bid >> 3;
    STAMP(p.ws, 0, 0);
    if (bid == 0 && tid < 128) ((unsigned*)(p.ws + WS_FLAG))[tid * 16] = 0u;
    const int wr = wid >> 2, wc = wid & 3;
    const int pm = idx & 7, pn = 2 * xcd + ((idx >> 3) & 1), kind = idx >> 4;
    f32x16 acc[4][2];
    auto store_f = [&](char* base, float sc) {
#pragma unroll
        for (int m = 0; m < 4; ++m)
#pragma unroll
            for (int n = 0; n < 2; ++n) { const int tb = pm * 8 + wr * 4 + m, fb = pn * 8 + wc * 2 + n, hd = fb >> 2, db = fb & 3;
                char* dst = base + (size_t)((hd * NTB + tb) * 8 + 2 * db) * 1024 + lane * 16;
                st16(dst, pack8(acc[m][n], 0, sc)); st16(dst + 1024, pack8(acc[m][n], 8, sc)); }
    };
    if (kind == 0) {
        KvUnit kv; const int tbk = xcd * 8 + pm, isv = (idx >> 3) & 1;
        big_stream<false>(lds, (const char*)p.xB + (size_t)pm * 256 * 2048, 2048, (const char*)p.wqB + pn * 512, nullptr, acc,
                          [&](int part) { kv.issue(part, lds, p, tbk, isv, wid, lane); }, wid, lane);
        SBAR();
        store_f(p.ws + WS_QB, CB);
        SBAR();
        kv.finish(lds, p, tbk, isv, wid, lane);
    } else {
        big_stream<true>(lds, (const char*)p.cKV + (size_t)pm * 256 * 1024, 1024, (const char*)p.wukB + pn * 512, (const char*)p.wuvB + pn * 512, acc,
                         [&](int) { store_f(p.ws + WS_KB, 1.f); }, wid, lane);
#pragma unroll
        for (int m = 0; m < 4; ++m)
#pragma unroll
            for (int n = 0; n < 2; ++n) { const int tb = pm * 8 + wr * 4 + m, fb = pn * 8 + wc * 2 + n, hd = fb >> 2, db = fb & 3;
                char* dst = p.ws + WS_VB + (size_t)((hd * NTB + tb) * 8 + db) * 1024 + lane * 16;
                st16(dst, pack8(acc[m][n], 0, 1.f)); st16(dst + 4096, pack8(acc[m][n], 8, 1.f)); }
    }
    STAMP(p.ws, 0, 1); STAMP(p.ws, 0, 2); STAMP(p.ws, 0, 3);
}

struct P2 { const u16 *xA, *wqA; char* ws; };

DI void exp16(const f32x16& s, unsigned (&pk)[8], float& t0, float& t1, unsigned& orv) {
#pragma unroll
    for (int i = 0; i < 8; ++i) {
        const float a = __builtin_amdgcn_exp2f(s[2 * i]), b = __builtin_amdgcn_exp2f(s[2 * i + 1]);
        t0 += a; t1 += b; pk[i] = cvtpk(a, b); orv |= pk[i];
    }
}
DI float max16(const f32x16& s) {
    float m = fmaxf(s[0], s[1]);
#pragma unroll
    for (int i = 2; i < 16; ++i) m = fmaxf(m, s[i]);
    return m;
}
DI void diag_mask(f32x16& s, int r, int h) {
#pragma unroll
    for (int i = 0; i < 16; ++i) { const int kr = (i & 3) + 8 * (i >> 2) + 4 * h; if (kr > r) s[i] = -__builtin_inff(); }
}
DI bf16x8 pfrag(const unsigned (&pk)[8], int b) { u32x4 w = {pk[b], pk[b + 1], pk[b + 2], pk[b + 3]}; return __builtin_bit_cast(bf16x8, w); }

#define XEXP(x) __builtin_amdgcn_exp2f(x)
DI void attnB(LAS char* lds, char* ws, int hd, int xb, int wid, int lane) {
    const int r = lane & 31, h = lane >> 5;
    const int tbq = 4 * xb + wid, NT = 2 * (xb + 1);
    const char* kbase = ws + WS_KB + (size_t)hd * NTB * 8192 + wid * 4096;
    const char* vbase = ws + WS_VB + (size_t)hd * NTB * 8192 + wid * 4096;
    const unsigned lds0 = (unsigned)(size_t)lds + wid * 4096, lane16 = lane * 16;
    auto piece = [&](const char* src, unsigned m0v, int i) { if (i == 0) glds16o<0>(src, lane16, m0v); else if (i == 1) glds16o<1024>(src, lane16, m0v); else if (i == 2) glds16o<2048>(src, lane16, m0v); else glds16o<3072>(src, lane16, m0v); };
    glds16x4(kbase, lane * 16, lds + wid * 4096); glds16x4(vbase, lane * 16, lds + 32768 + wid * 4096); glds16x4(kbase + 16384, lane * 16, lds + 16384 + wid * 4096);
    bf16x8 qf[8];
#pragma unroll
    for (int s = 0; s < 8; ++s) qf[s] = *(const bf16x8*)(ws + WS_QB + (size_t)((hd * NTB + tbq) * 8 + s) * 1024 + lane * 16);
    f32x16 o[4], negm, S0, S1; float l = 0.f, t0, t1;
    bf16x8 fr[8]; unsigned pk[8], pn[4];
#pragma unroll
    for (int d = 0; d < 4; ++d) o[d] = f32x16{};
    LAS const char* lbase = lds + lane * 16;
    auto kaddr = [&](int j) { return lbase + ((j >> 1) & 1) * 16384 + (j & 1) * 8192; };
    auto vaddr = [&](int j) { return lbase + 32768 + ((j >> 1) & 1) * 16384 + (j & 1) * 8192; };
    auto slotA = [&](auto ST, f32x16& Sc, f32x16& Sn, int j, const char* src, unsigned m0v) {
        LAS const char* va = vaddr(j);
        float ea = 0.f, eb = 0.f;
#pragma unroll
        for (int i = 0; i < 4; ++i) pk[i] = pn[i];
#pragma unroll
        for (int i = 0; i < 8; ++i) {
            Sn = mfma32(fr[i], qf[i], i ? Sn : negm);
            fr[i] = lds_b128(va + i * 1024);
            if (!(i & 1)) { ea = XEXP(Sc[8 + i]); eb = XEXP(Sc[9 + i]); PIN(ea); PIN(eb); if (ST.value) piece(src, m0v, i >> 1); }
            else { pk[4 + (i >> 1)] = cvtpk(ea, eb); t0 += ea; t1 += eb; PIN(t0); PIN(t1); }
            SBAR();
        }
        float ts = t0 + t1;
        if (__builtin_expect(__any(!(ts < 1.f)), 0)) {
            const float delta = fmaxf(swap32_max(max16(Sc)) + SM_BIAS, 0.f), alpha = __builtin_amdgcn_exp2f(-delta);
            l *= alpha;
#pragma unroll
            for (int d = 0; d < 4; ++d) o[d] *= alpha;
            negm -= delta; Sn -= delta; t0 = 0.f; t1 = 0.f;
#pragma unroll
            for (int i = 0; i < 8; ++i) { const float a = __builtin_amdgcn_exp2f(Sc[2 * i] - delta), b = __builtin_amdgcn_exp2f(Sc[2 * i + 1] - delta); pk[i] = cvtpk(a, b); t0 += a; t1 += b; }
            ts = t0 + t1;
        }
        l += ts;
        if (j + 1 == tbq) diag_mask(Sn, r, h);
    };
    auto slotB = [&](auto ST, f32x16& Sn, int j, const char* src, unsigned m0v) {
        LAS const char* ka = kaddr(j + 2);
        float ea = 0.f, eb = 0.f;
        t0 = 0.f; t1 = 0.f;
#pragma unroll
        for (int i = 0; i < 8; ++i) {
            o[i & 3] = mfma32(fr[i], pfrag(pk, 4 * (i >> 2)), o[i & 3]);
            fr[i] = lds_b128(ka + i * 1024);
            if (!(i & 1)) { if (ST.value) piece(src, m0v, i >> 1); ea = XEXP(Sn[i]); eb = XEXP(Sn[i + 1]); PIN(ea); PIN(eb); }
            else { pn[i >> 1] = cvtpk(ea, eb); t0 += ea; t1 += eb; PIN(pn[i >> 1]); PIN(t0); PIN(t1); }
            SBAR();
        }
    };
    using TT = std::integral_constant<bool, true>; using FF = std::integral_constant<bool, false>;
    WAIT_BAR(0);
    {
        S0 = f32x16{};
#pragma unroll
        for (int s = 0; s < 8; ++s) fr[s] = lds_b128(lbase + s * 1024);
#pragma unroll
        for (int s = 0; s < 8; ++s) S0 = mfma32(fr[s], qf[s], S0);
#pragma unroll
        for (int s = 0; s < 8; ++s) fr[s] = lds_b128(lbase + 8192 + s * 1024);
        if (tbq == 0) diag_mask(S0, r, h);
        const float nm = -(swap32_max(max16(S0)) + SM_BIAS);
#pragma unroll
        for (int i = 0; i < 16; ++i) negm[i] = nm;
        S0 += nm;
        t0 = 0.f; t1 = 0.f;
#pragma unroll
        for (int i = 0; i < 4; ++i) { const float a = __builtin_amdgcn_exp2f(S0[2 * i]), b = __builtin_amdgcn_exp2f(S0[2 * i + 1]); pn[i] = cvtpk(a, b); t0 += a; t1 += b; }
    }
    for (int j = 0;; j += 2) {
        const int t = j >> 1;
        WAIT_BAR(0);
#define BPRIO_MASK 3
        if ((t & BPRIO_MASK) != BPRIO_MASK || BPRIO_MASK == 0) __builtin_amdgcn_s_setprio(2); else __builtin_amdgcn_s_setprio(0);
        const int tk = t + 2 < NT ? t + 2 : NT - 1, tv = t + 1 < NT ? t + 1 : NT - 1;
        slotA(TT{}, S0, S1, j, kbase + (size_t)tk * 16384, lds0 + (t & 1) * 16384);  slotB(TT{}, S1, j, vbase + (size_t)tv * 16384, lds0 + 32768 + ((t + 1) & 1) * 16384);
        if (j == tbq) break;
        slotA(FF{}, S1, S0, j + 1, nullptr, 0u); slotB(FF{}, S0, j + 1, nullptr, 0u);
        if (j + 1 == tbq) break;
    }
    if (wid < 2) WAIT_BAR(0);
    asm volatile("s_waitcnt vmcnt(0)" ::: "memory");
    const float inv = 1.f / swap32_add(l);
    char* dst = ws + WS_CTX + (size_t)(tbq * 256 + hd * 8) * 1024 + lane * 16;
#pragma unroll
    for (int d = 0; d < 4; ++d) { st16(dst + (2 * d) * 1024, pack8(o[d], 0, inv)); st16(dst + (2 * d + 1) * 1024, pack8(o[d], 8, inv)); }
}

DI void projA(LAS char* lds, const P2& p, int hg, int tg, bf16x8 (&qf)[2][2], int wid, int lane) {
    constexpr int STG = 24576, NST = 16;
    const int r = lane & 31, h = lane >> 5;
    const char* xsrc = (const char*)p.xA + (size_t)tg * 64 * 2048;
    unsigned voX[2], voW[4];
#pragma unroll
    for (int i = 0; i < 2; ++i) { const int row = (wid * 2 + i) * 8 + (lane >> 3), ch = (lane & 7) ^ ((row >> 1) & 7); voX[i] = (unsigned)row * 2048u + ch * 16; }
#pragma unroll
    for (int i = 0; i < 4; ++i) { const int k = ((wid & 1) * 4 + i) * 8 + (lane >> 3), slot = lane & 7;
        voW[i] = (unsigned)k * 2048u + (128 * hg + 64 * (wid >> 1) + 32 * ((slot >> 2) ^ ((k >> 1) & 1)) + 8 * (slot & 3)) * 2; }
    auto stage1 = [&](int slot, int t, int i) {
        LAS char* base = lds + slot * STG;
        if (i < 2) glds16(xsrc + (size_t)t * 128, voX[i], base + (wid * 2 + i) * 1024);
        else glds16((const char*)p.wqA + (size_t)t * 64 * 2048, voW[i - 2], base + 8192 + (wid >> 1) * 8192 + ((wid & 1) * 4 + i - 2) * 1024);
    };
    int aoff[4];
#pragma unroll
    for (int s = 0; s < 4; ++s) aoff[s] = r * 128 + (((2 * s + h) ^ ((r >> 1) & 7)) << 4);
    const int blk = (lane >> 4) & 1, q = (lane & 15) >> 2, pq = lane & 3;
    const int boff = 8192 + (wid >> 1) * 8192 + (8 * h + q) * 128 + 64 * ((wid & 1) ^ (q >> 1)) + 32 * blk + 8 * pq;
    f32x16 acc[2]; acc[0] = f32x16{}; acc[1] = f32x16{};
    bf16x8 wf[4], a0[4], a1[4];
    auto rd = [&](int slot, int s) { LAS const char* sa = lds + slot * STG;
        wf[s] = cat8(lds_tr(sa + boff + s * 2048), lds_tr(sa + boff + s * 2048 + 512)); a0[s] = lds_b128(sa + aoff[s]); a1[s] = lds_b128(sa + aoff[s] + 4096); };
#pragma unroll
    for (int t = 0; t < 3; ++t)
#pragma unroll
        for (int i = 0; i < 6; ++i) stage1(t, t, i);
    WAIT_BAR(12);
    rd(0, 0); rd(0, 1);
    int slot = 0;
    for (int t = 0; t < NST; ++t) {
        int sn = slot + 1; if (sn == 3) sn = 0;
        rd(slot, 2); acc[0] = mfma32(wf[0], a0[0], acc[0]); acc[1] = mfma32(wf[0], a1[0], acc[1]); SBAR();
        rd(slot, 3); acc[0] = mfma32(wf[1], a0[1], acc[0]); acc[1] = mfma32(wf[1], a1[1], acc[1]); SBAR();
        if (t + 2 < NST) { WAIT_BAR(6); } else { WAIT_BAR(0); }
        rd(sn, 0); acc[0] = mfma32(wf[2], a0[2], acc[0]); acc[1] = mfma32(wf[2], a1[2], acc[1]); SBAR();
        if (t + 3 < NST) { stage1(slot, t + 3, 0); stage1(slot, t + 3, 1); stage1(slot, t + 3, 2); }
        rd(sn, 1); acc[0] = mfma32(wf[3], a0[3], acc[0]); acc[1] = mfma32(wf[3], a1[3], acc[1]); SBAR();
        if (t + 3 < NST) { stage1(slot, t + 3, 3); stage1(slot, t + 3, 4); stage1(slot, t + 3, 5); }
        slot = sn;
    }
#pragma unroll
    for (int u = 0; u < 2; ++u)
#pragma unroll
        for (int s = 0; s < 2; ++s) qf[u][s] = __builtin_bit_cast(bf16x8, pack8(acc[u], 8 * s, CA));
    WAIT_BAR(0);
}
DI void attnA(LAS char* lds, const P2& p, int hg, int tg, int wid, int lane) {
    char* ws = p.ws;
    const int hd = 4 * hg + wid, tb0 = 2 * tg;
    bf16x8 qf[2][2];
    __builtin_amdgcn_s_setprio(3);
    projA(lds, p, hg, tg, qf, wid, lane);
    STAMP(ws, 1, 1);
    __builtin_amdgcn_s_setprio(1);
    STAMP(ws, 1, 2);
    const char* kvsrc = ws + ((wid & 2) ? WS_VA : WS_KA) + (wid & 1) * 4096;
    auto stage = [&](int slot, int t) { glds16x4(kvsrc + (size_t)t * 8192, lane * 16, lds + slot * 16384 + wid * 4096); };
    stage(0, 0); stage(1, 1);
    f32x16 o[2], negm[2], Sa[2], Sb[2]; float l[2] = {0.f, 0.f};
    o[0] = f32x16{}; o[1] = f32x16{};
    bf16x8 kf[2], vf[2]; unsigned pp[2][8];
    LAS const char* lbase = lds + lane * 16;
    auto step = [&](f32x16 (&Sc)[2], f32x16 (&Sn)[2], LAS const char* ka, LAS const char* va) {
        unsigned pn[2][8]; float t[2][2] = {{0.f, 0.f}, {0.f, 0.f}}, e[2][4];
#pragma unroll
        for (int i = 0; i < 9; ++i) {
            if (i < 8) {
                const int u = i & 1, s = (i >> 1) & 1;
                if (i < 4) Sn[u] = mfma32(kf[s], qf[u][s], s ? Sn[u] : negm[u]);
                else o[u] = mfma32(vf[s], pfrag(pp[u], 4 * s), o[u]);
                if (i == 3) { kf[0] = lds_b128(ka); kf[1] = lds_b128(ka + 1024); }
                if (i == 7) { vf[0] = lds_b128(va); vf[1] = lds_b128(va + 1024); }
#pragma unroll
                for (int k = 0; k < 4; ++k) { e[i & 1][k] = __builtin_amdgcn_exp2f(Sc[i >> 2][(i & 3) * 4 + k]); PIN(e[i & 1][k]); }
            }
            if (i > 0) {
                const int g = i - 1, uu = g >> 2, w = (g & 3) * 2; const float* x = e[g & 1];
                pn[uu][w] = cvtpk(x[0], x[1]); pn[uu][w + 1] = cvtpk(x[2], x[3]);
                t[uu][0] += x[0]; t[uu][1] += x[1]; t[uu][0] += x[2]; t[uu][1] += x[3]; PIN(t[uu][0]); PIN(t[uu][1]); PIN(pn[uu][w]); PIN(pn[uu][w + 1]);
            }
            if (i < 8) SBAR();
        }
        float ts[2] = {t[0][0] + t[0][1], t[1][0] + t[1][1]};
        if (__builtin_expect(__any(!(ts[0] + ts[1] < 1.f)), 0)) {
#pragma unroll
            for (int u = 0; u < 2; ++u) {
                const float dl = fmaxf(swap32_max(max16(Sc[u])) + SM_BIAS, 0.f), al = __builtin_amdgcn_exp2f(-dl);
                l[u] *= al; o[u] *= al; negm[u] -= dl; Sn[u] -= dl; float a0 = 0.f, a1 = 0.f;
#pragma unroll
                for (int i = 0; i < 8; ++i) { const float a = __builtin_amdgcn_exp2f(Sc[u][2 * i] - dl), b = __builtin_amdgcn_exp2f(Sc[u][2 * i + 1] - dl); pn[u][i] = cvtpk(a, b); a0 += a; a1 += b; }
                ts[u] = a0 + a1;
            }
        }
#pragma unroll
        for (int u = 0; u < 2; ++u) { l[u] += ts[u];
#pragma unroll
            for (int i = 0; i < 8; ++i) pp[u][i] = pn[u][i]; }
    };
    WAIT_BAR(4);
    {
        kf[0] = lds_b128(lbase); kf[1] = lds_b128(lbase + 1024);
#pragma unroll
        for (int u = 0; u < 2; ++u) {
            Sa[u] = mfma32(kf[0], qf[u][0], f32x16{}); Sa[u] = mfma32(kf[1], qf[u][1], Sa[u]);
            const float nm = -(swap32_max(max16(Sa[u])) + SM_BIAS);
#pragma unroll
            for (int i = 0; i < 16; ++i) negm[u][i] = nm;
            Sa[u] += nm;
#pragma unroll
            for (int i = 0; i < 8; ++i) pp[u][i] = 0u;
        }
        kf[0] = lds_b128(lbase + 2048); kf[1] = lds_b128(lbase + 2048 + 1024);
        vf[0] = kf[0]; vf[1] = kf[1];
    }
    int slot = 0;
    for (int t = 0; t < 16; ++t) {
        WAIT_BAR(0);
#define APRIO_T 99
        if (t == APRIO_T) __builtin_amdgcn_s_setprio(3);
        int sn = slot + 1; if (sn == 3) sn = 0; int sp = sn + 1; if (sp == 3) sp = 0;
        if (t + 2 < 16) stage(sp, t + 2);
        LAS const char* cur = lbase + slot * 16384; LAS const char* nxt = lbase + sn * 16384;
        step(Sa, Sb, cur + 2 * 2048, cur + 8192);
        step(Sb, Sa, cur + 3 * 2048, cur + 8192 + 2048);
        step(Sa, Sb, nxt, cur + 8192 + 2 * 2048);
        step(Sb, Sa, nxt + 2048, cur + 8192 + 3 * 2048);
        slot = sn;
    }
#pragma unroll
    for (int s = 0; s < 2; ++s)
#pragma unroll
        for (int u = 0; u < 2; ++u) o[u] = mfma32(vf[s], pfrag(pp[u], 4 * s), o[u]);
#pragma unroll
    for (int u = 0; u < 2; ++u) {
        const float inv = 1.f / swap32_add(l[u]);
        char* dst = ws + WS_OA + (size_t)((tb0 + u) * 64 + hd * 2) * 1024 + lane * 16;
        st16(dst, pack8(o[u], 0, inv)); st16(dst + 1024, pack8(o[u], 8, inv));
    }
}

__global__ __launch_bounds__(256, 2) void k_attn(P2 p) {
    extern __shared__ __attribute__((aligned(16))) char smem[];
    LAS char* lds = (LAS char*)smem;
    const int tid = threadIdx.x, wid = __builtin_amdgcn_readfirstlane(tid >> 6), lane = tid & 63;
    const int bid = blockIdx.x;
    const int xcd = bid & 7, idx = bid >> 3,
        kind = (idx ^ (idx >> 5)) & 1,
        it = (idx >> 1) & 15 | (idx >> 5) << 4;
    STAMP(p.ws, 1, 0);
    if (kind == 0) { attnA(lds, p, xcd, it, wid, lane); }
    else { const int hd = xcd + 8 * (it >> 3), x = it & 7;
        attnB(lds, p.ws, hd, 15 - x, wid, lane);
        WAIT_BAR(0);
        STAMP(p.ws, 1, 1);
        attnB(lds, p.ws, hd, x, wid, lane); STAMP(p.ws, 1, 2); }
    STAMP(p.ws, 1, 3);
}

struct P3 { const u16 *woA, *woB; char* ws; u16* out; };

constexpr int NT1 = 24;
DI void out_store(u16* out, int tb0, int col0, const f32x16 (&acc)[2][2], int wid, int lane) {
    const int wm = wid >> 1, wn = wid & 1, r = lane & 31, h = lane >> 5;
#pragma unroll
    for (int m = 0; m < 2; ++m) {
        char* dst = (char*)out + (size_t)((tb0 + 2 * wm + m) * 32 + r) * 4096 + (col0 + 64 * wn + 8 * h) * 2;
#pragma unroll
        for (int c = 0; c < 2; ++c) { *(u32x4*)(dst + c * 64) = pack8(acc[m][c], 0, 1.f); *(u32x4*)(dst + c * 64 + 32) = pack8(acc[m][c], 8, 1.f); }
    }
}

__global__ __launch_bounds__(512, 2) void k_out(P3 p) {
    extern __shared__ __attribute__((aligned(16))) char smem[];
    LAS char* lds = (LAS char*)smem;
    constexpr int STG = 32768, nt = 40;
    const int tid = threadIdx.x, wid = __builtin_amdgcn_readfirstlane(tid >> 6), lane = tid & 63;
    const int bid = blockIdx.x, xg = bid & 7, role = (xg >> 2) ^ 1, ti = bid >> 3;
    const int pm = 8 * ((xg >> 1) & 1) + (ti >> 2), pn = 4 * (xg & 1) + (ti & 3), tb0 = pm * 4, tau = pm * 8 + pn;
    STAMP(p.ws, 2, 0);
    if (wid >= 4) {
        const int lw = wid - 4;
        const char* a0 = p.ws + WS_CTX + (size_t)((tb0 + lw) * 256 + (role ? 4 * (64 - NT1) : 0)) * 1024;
        const char* w0 = (const char*)p.woB + pn * 256 + (size_t)(role ? 64 - NT1 : 0) * 64 * 2048;
        const char* a1 = p.ws + WS_OA + (size_t)((tb0 + lw) * 64) * 1024 - (size_t)NT1 * 4096;
        const char* w1 = (const char*)p.woA + pn * 256 - (size_t)NT1 * 64 * 2048;
        const int tsw = role ? NT1 : nt;
        unsigned voW[4];
#pragma unroll
        for (int i = 0; i < 4; ++i) { const int pc = 4 * lw + i, g = pc >> 3, k = (pc & 7) * 8 + (lane >> 3), slot = lane & 7;
            const int col = 64 * g + 32 * ((slot >> 2) ^ ((k >> 1) & 1)) + 8 * (slot & 3); voW[i] = (unsigned)k * 2048u + col * 2; }
        auto stage = [&](int t) {
            LAS char* base = lds + (t & 3) * STG;
            const char* as = (t < tsw ? a0 : a1) + (size_t)t * 4096; const char* wsd = (t < tsw ? w0 : w1) + (size_t)t * (64 * 2048);
            glds16x4(as, lane * 16, base + lw * 4096);
#pragma unroll
            for (int i = 0; i < 4; ++i) glds16(wsd, voW[i], base + 16384 + (4 * lw + i) * 1024);
        };
#pragma unroll
        for (int t = 0; t < 4; ++t) stage(t);
        WAIT_BAR(24);
        for (int t = 0; t < nt; ++t) {
            if (t + 3 < nt) { WAIT_BAR(16); } else if (t + 2 < nt) { WAIT_BAR(8); } else { WAIT_BAR(0); }
            if (t + 4 < nt) stage(t + 4);
        }
    } else {
        const int wm = wid >> 1, wn = wid & 1, h = lane >> 5;
        const int blk = (lane >> 4) & 1, q = (lane & 15) >> 2, pq = lane & 3, pp = ((pq & 1) << 1) | (pq >> 1);
        GAS unsigned* flag = (GAS unsigned*)(p.ws + WS_FLAG) + tau * 16;
        const __amdgpu_buffer_rsrc_t rs = __builtin_amdgcn_make_buffer_rsrc(p.ws + WS_PART + (size_t)tau * 65536, 0, 65536, 0x00020000);
        int boff[2];
#pragma unroll
        for (int c = 0; c < 2; ++c) boff[c] = 16384 + wn * 8192 + (4 * h + q) * 128 + 64 * (c ^ (q >> 1)) + 32 * blk + 8 * pp;
        const int aoff = (2 * wm * 4) * 1024 + lane * 16;
        f32x16 acc[2][2];
#pragma unroll
        for (int m = 0; m < 2; ++m) { acc[m][0] = f32x16{}; acc[m][1] = f32x16{}; }
        bf16x8 af[4][2], wf[4][2];
        auto rd = [&](int slot, int s, int i) {
            LAS const char* sa = lds + slot * STG;
            if (i & 1) af[s][i >> 1] = lds_b128(sa + aoff + ((i >> 1) * 4 + s) * 1024);
            else wf[s][i >> 1] = cat8(lds_tr(sa + boff[i >> 1] + s * 2048), lds_tr(sa + boff[i >> 1] + s * 2048 + 1024));
        };
        auto kstep = [&](int s, int nslot) {
#pragma unroll
            for (int i = 0; i < 4; ++i) {
                const int m = i >> 1, c = i & 1;
                acc[m][c] = mfma32(wf[s][c], af[s][m], acc[m][c]);
                rd(nslot, (s + 2) & 3, i);
                SBAR();
            }
        };
        auto tiles = [&](int t0, int t1) {
#pragma unroll 1
            for (int t = t0; t < t1; ++t) {
                kstep(0, t & 3); kstep(1, t & 3);
                asm volatile("s_waitcnt lgkmcnt(0)\n\ts_barrier" ::: "memory");
                kstep(2, (t + 1) & 3); kstep(3, (t + 1) & 3);
            }
        };
        WAIT_BAR(0);
#pragma unroll
        for (int i = 0; i < 4; ++i) { rd(0, 0, i); rd(0, 1, i); }
        if (role == 1) {
            tiles(0, NT1);
#pragma unroll
            for (int m = 0; m < 2; ++m)
#pragma unroll
                for (int c = 0; c < 2; ++c) {
#pragma unroll
                    for (int i = 0; i < 4; ++i) { u32x4 v;
#pragma unroll
                        for (int j = 0; j < 4; ++j) v[j] = __float_as_uint(acc[m][c][4 * i + j]);
                        __builtin_amdgcn_raw_buffer_store_b128(v, rs, wid * 16384 + ((m * 2 + c) * 4 + i) * 1024 + lane * 16, 0, 16); }
                    acc[m][c] = f32x16{}; }
            tiles(NT1, NT1 + 4);
            asm volatile("s_waitcnt vmcnt(0)" ::: "memory");
            tiles(NT1 + 4, NT1 + 5);
            if (tid == 0) __hip_atomic_store(flag, 1u, __ATOMIC_RELAXED, __HIP_MEMORY_SCOPE_AGENT);
            STAMP(p.ws, 2, 1);
            tiles(NT1 + 5, nt);
            STAMP(p.ws, 2, 2);
            out_store(p.out, tb0, pn * 128, acc, wid, lane);
        } else {
            tiles(0, nt - 5);
            while ((unsigned)__builtin_amdgcn_readfirstlane(__hip_atomic_load(flag, __ATOMIC_RELAXED, __HIP_MEMORY_SCOPE_AGENT)) != 1u) __builtin_amdgcn_s_sleep(2);
            __builtin_amdgcn_fence(__ATOMIC_ACQUIRE, "wavefront");
            u32x4 pv[16];
#pragma unroll
            for (int i = 0; i < 16; ++i) pv[i] = __builtin_amdgcn_raw_buffer_load_b128(rs, wid * 16384 + i * 1024 + lane * 16, 0, 16);
            STAMP(p.ws, 2, 1);
            tiles(nt - 5, nt);
#pragma unroll
            for (int i = 0; i < 16; ++i)
#pragma unroll
                for (int j = 0; j < 4; ++j) acc[i >> 3][(i >> 2) & 1][4 * (i & 3) + j] += __uint_as_float(pv[i][j]);
            STAMP(p.ws, 2, 2);
            out_store(p.out, tb0, 1024 + pn * 128, acc, wid, lane);
        }
        STAMP(p.ws, 2, 3);
    }
}

#define BENCH_MARK(s)
extern "C" void kernel_launch(void* const* d_in, const int* in_sizes, int n_in, void* d_out, int out_size, void* d_ws, size_t ws_size,
                              hipStream_t stream) {
    (void)in_sizes; (void)n_in; (void)out_size; (void)ws_size;
    (void)hipFuncSetAttribute((const void*)k_proj, hipFuncAttributeMaxDynamicSharedMemorySize, 133120);
    (void)hipFuncSetAttribute((const void*)k_attn, hipFuncAttributeMaxDynamicSharedMemorySize, 73728);
    (void)hipFuncSetAttribute((const void*)k_out, hipFuncAttributeMaxDynamicSharedMemorySize, 131072);
    P1 p1; p1.xA = (const u16*)d_in[0]; p1.wkA = (const u16*)d_in[2]; p1.wvA = (const u16*)d_in[3];
    p1.xB = (const u16*)d_in[5]; p1.cKV = (const u16*)d_in[6]; p1.wqB = (const u16*)d_in[7]; p1.wukB = (const u16*)d_in[8]; p1.wuvB = (const u16*)d_in[9];
    p1.ws = (char*)d_ws;
    BENCH_MARK(stream);
    k_proj<<<256, 512, 133120, stream>>>(p1);
    BENCH_MARK(stream);
    P2 p2; p2.xA = (const u16*)d_in[0]; p2.wqA = (const u16*)d_in[1]; p2.ws = (char*)d_ws;
    k_attn<<<512, 256, 73728, stream>>>(p2);
    BENCH_MARK(stream);
    P3 p3; p3.woA = (const u16*)d_in[4]; p3.woB = (const u16*)d_in[10]; p3.ws = (char*)d_ws; p3.out = (u16*)d_out;
    k_out<<<256, 512, 131072, stream>>>(p3);
    BENCH_MARK(stream);
}
```

```cpp
#include <hip/hip_runtime.h>
#include <hip/hip_bf16.h>
#include <type_traits>

typedef unsigned short u16;
typedef short bf16x8 __attribute__((ext_vector_type(8)));
typedef short bf16x4 __attribute__((ext_vector_type(4)));
typedef float f32x16 __attribute__((ext_vector_type(16)));
typedef float f32x4 __attribute__((ext_vector_type(4)));
typedef float f32x2 __attribute__((ext_vector_type(2)));
typedef __bf16 bf16x2_t __attribute__((ext_vector_type(2)));
typedef unsigned u32x4 __attribute__((ext_vector_type(4)));
#define LAS __attribute__((address_space(3)))
#define GAS __attribute__((address_space(1)))
#define DI __device__ __forceinline__

DI f32x16 mfma32(bf16x8 a, bf16x8 b, f32x16 c) { return __builtin_amdgcn_mfma_f32_32x32x16_bf16(a, b, c, 0, 0, 0); }
DI unsigned cvtpk(float lo, float hi) { f32x2 v = {lo, hi}; bf16x2_t b = __builtin_convertvector(v, bf16x2_t); return __builtin_bit_cast(unsigned, b); }
DI void glds16(const void* sbase, unsigned voff, LAS void* l) {
    unsigned sv; asm volatile("s_mov_b32 %0, m0\n\ts_mov_b32 m0, %3\n\ts_nop 0\n\tglobal_load_lds_dwordx4 %1, %2\n\ts_mov_b32 m0, %0"
                              : "=&s"(sv) : "v"(voff), "s"(sbase), "s"((unsigned)(size_t)l) : "memory"); }
template <int OFF> DI void glds16o(const void* sbase, unsigned voff, unsigned m0v) {
    asm volatile("s_mov_b32 m0, %2\n\ts_nop 0\n\tglobal_load_lds_dwordx4 %0, %1 offset:%3" :: "v"(voff), "s"(sbase), "s"(m0v), "i"(OFF) : "memory"); }
DI void l2pf(const void* sbase, unsigned voff, LAS void* scratch) {
    unsigned sv; asm volatile("s_mov_b32 %0, m0\n\ts_mov_b32 m0, %3\n\ts_nop 0\n\tglobal_load_lds_dword %1, %2\n\ts_mov_b32 m0, %0"
                              : "=&s"(sv) : "v"(voff), "s"(sbase), "s"((unsigned)(size_t)scratch) : "memory"); }
DI void glds16v(const void* vaddr, LAS void* l) {
    unsigned sv; asm volatile("s_mov_b32 %0, m0\n\ts_mov_b32 m0, %2\n\ts_nop 0\n\tglobal_load_lds_dwordx4 %1, off\n\ts_mov_b32 m0, %0"
                              : "=&s"(sv) : "v"(vaddr), "s"((unsigned)(size_t)l) : "memory"); }
DI void glds16x4(const void* sbase, unsigned voff, LAS void* l) {
    unsigned sv; asm volatile("s_mov_b32 %0, m0\n\ts_mov_b32 m0, %3\n\ts_nop 0\n\tglobal_load_lds_dwordx4 %1, %2\n\tglobal_load_lds_dwordx4 %1, %2 offset:1024\n\t"
                              "global_load_lds_dwordx4 %1, %2 offset:2048\n\tglobal_load_lds_dwordx4 %1, %2 offset:3072\n\ts_mov_b32 m0, %0"
                              : "=&s"(sv) : "v"(voff), "s"(sbase), "s"((unsigned)(size_t)l) : "memory"); }
DI void glds16x2(const void* sbase, unsigned voff, LAS void* l) {
    unsigned sv; asm volatile("s_mov_b32 %0, m0\n\ts_mov_b32 m0, %3\n\ts_nop 0\n\tglobal_load_lds_dwordx4 %1, %2\n\tglobal_load_lds_dwordx4 %1, %2 offset:1024\n\ts_mov_b32 m0, %0"
                              : "=&s"(sv) : "v"(voff), "s"(sbase), "s"((unsigned)(size_t)l) : "memory"); }
DI bf16x8 lds_b128(LAS const char* p) { return *(LAS const bf16x8*)p; }
DI bf16x4 lds_tr(LAS const char* p) { return __builtin_amdgcn_ds_read_tr16_b64_v4i16((LAS bf16x4*)p); }
DI bf16x8 cat8(bf16x4 a, bf16x4 b) { return (bf16x8){a[0], a[1], a[2], a[3], b[0], b[1], b[2], b[3]}; }
DI float swap32_max(float x) { auto rr = __builtin_amdgcn_permlane32_swap(__float_as_uint(x), __float_as_uint(x), false, false); return fmaxf(__uint_as_float(rr[0]), __uint_as_float(rr[1])); }
DI float swap32_add(float x) { auto rr = __builtin_amdgcn_permlane32_swap(__float_as_uint(x), __float_as_uint(x), false, false); return __uint_as_float(rr[0]) + __uint_as_float(rr[1]); }
#define WAIT_BAR(N) asm volatile("s_waitcnt vmcnt(" #N ") lgkmcnt(0)\n\ts_barrier" ::: "memory")
#define SBAR() __builtin_amdgcn_sched_barrier(0)
#define PIN(x) asm volatile("" : "+v"(x))
#define WAIT_BAR_I(N) asm volatile("s_waitcnt vmcnt(%0) lgkmcnt(0)\n\ts_barrier" :: "i"(N) : "memory")

DI void st16(void* p, u32x4 v) { *(u32x4*)p = v; }
DI u32x4 pack8(const f32x16& a, int b, float sc) {
    u32x4 w; w.x = cvtpk(a[b + 0] * sc, a[b + 1] * sc); w.y = cvtpk(a[b + 2] * sc, a[b + 3] * sc);
    w.z = cvtpk(a[b + 4] * sc, a[b + 5] * sc); w.w = cvtpk(a[b + 6] * sc, a[b + 7] * sc); return w;
}

constexpr int SEQ = 2048, NTB = SEQ / 32;
constexpr float LOG2E = 1.4426950408889634f;
constexpr float CA = 0.17677669529663687f * LOG2E;
constexpr float CB = 0.08838834764831845f * LOG2E;
#define SMB 64.f
constexpr float SM_BIAS = SMB;

constexpr size_t WS_QB = 0, WS_KB = WS_QB + (16u << 20), WS_VB = WS_KB + (16u << 20), WS_QA = WS_VB + (16u << 20),
                 WS_KA = WS_QA + (4u << 20), WS_VA = WS_KA + (128u << 10), WS_CTX = WS_VA + (128u << 10), WS_OA = WS_CTX + (16u << 20),
                 WS_PART = WS_OA + (4u << 20), WS_FLAG = WS_PART + (8u << 20), WS_END = WS_FLAG + 129 * 64;

#define STAMP(ws, kn, i)
struct P1 { const u16 *xB, *cKV, *wqB, *wukB, *wuvB, *xA, *wkA, *wvA, *wqA; char* ws; };

template <bool KV, class Mid>
DI void big_stream(LAS char* lds, const char* Ab, unsigned lda_b, const char* W0, const char* W1, f32x16 (&acc)[4][2], Mid mid, const char* warm, const char* warm2, int wid, int lane) {
    constexpr unsigned ldw_b = 8192; constexpr int ns = 32;
    const int wr = wid >> 2, wc = wid & 3, r = lane & 31, h = lane >> 5;
    unsigned voA[2], voW[2];
#pragma unroll
    for (int i = 0; i < 2; ++i) {
        const int row = (wid * 2 + i) * 16 + (lane >> 2), ch = (lane & 3) ^ ((row >> 2) & 3);
        voA[i] = (unsigned)row * lda_b + ch * 16;
        const int k = ((wid & 1) * 2 + i) * 8 + (lane >> 3), slot = lane & 7;
        const int col = 64 * (wid >> 1) + 32 * ((slot >> 2) ^ ((k >> 1) & 1)) + 8 * (slot & 3);
        voW[i] = (unsigned)k * ldw_b + col * 2;
    }
    int aoff[2], boff[2];
#pragma unroll
    for (int s = 0; s < 2; ++s) aoff[s] = (128 * wr + r) * 64 + (((2 * s + h) ^ ((r >> 2) & 3)) << 4);
    { const int blk = (lane >> 4) & 1, q = (lane & 15) >> 2, p = lane & 3;
#pragma unroll
      for (int n = 0; n < 2; ++n) boff[n] = 16384 + wc * 4096 + (8 * h + q) * 128 + 64 * (n ^ (q >> 1)) + 32 * blk + 8 * p; }
#pragma unroll
    for (int m = 0; m < 4; ++m) { acc[m][0] = f32x16{}; acc[m][1] = f32x16{}; }
    auto stage1 = [&](int t, int i) {
        LAS char* base = lds + (t & 3) * 32768;
        const int tk = KV ? (t & 15) : t;
        if (i < 2) glds16(Ab + (size_t)tk * 64, voA[i], base + (wid * 2 + i) * 1024);
        else glds16(((KV && t >= 16) ? W1 : W0) + (size_t)tk * 32 * ldw_b, voW[i - 2], base + 16384 + (wid >> 1) * 4096 + ((wid & 1) * 2 + i - 2) * 1024);
    };
    bf16x8 af[2][4], wf[2][2];
    auto rd = [&](int rs, int t, int s, int i) {
        LAS const char* sa = lds + (t & 3) * 32768;
        if (i < 2) wf[rs][i] = cat8(lds_tr(sa + boff[i] + s * 2048), lds_tr(sa + boff[i] + s * 2048 + 512));
        else af[rs][i - 2] = lds_b128(sa + aoff[s] + (i - 2) * 2048);
    };
    auto kstep = [&](auto OT, int rs, int nt_, int nstep, int st) {
#pragma unroll
        for (int i = 0; i < 8; ++i) {
            const int m = i >> 1, n = i & 1;
            acc[m][n] = OT.value ? mfma32(wf[rs][n], af[rs][m], acc[m][n]) : mfma32(af[rs][m], wf[rs][n], acc[m][n]);
            if (i < 6) rd(rs ^ 1, nt_, nstep, i);
            if (rs == 1 && (i & 1) && st < ns) stage1(st, i >> 1);
            SBAR();
        }
    };
    using TT = std::integral_constant<bool, true>; using FF = std::integral_constant<bool, false>;
#pragma unroll
    for (int t = 0; t < 4; ++t)
#pragma unroll
        for (int i = 0; i < 4; ++i) stage1(t, i);
    WAIT_BAR(12);
#pragma unroll
    for (int i = 0; i < 6; ++i) rd(0, 0, 0, i);
    for (int t = 0; t < (KV ? 16 : 28); ++t) {
        kstep(TT{}, 0, t, 1, 0);
        if (t == 13 || t == 14) { WAIT_BAR(10); } else { WAIT_BAR(8); }
        if (t == 12) {
            l2pf(warm, (lane < 24 ? lane : 0) * 128, lds + 131072 + wid * 256); l2pf(warm2, lane * 128, lds + 131072 + wid * 256); }
        kstep(TT{}, 1, t + 1, 0, t + 4);
    }
    if constexpr (!KV) {
        kstep(TT{}, 0, 28, 1, 0); WAIT_BAR(8);  mid(0); kstep(TT{}, 1, 29, 0, ns);
        kstep(TT{}, 0, 29, 1, 0); WAIT_BAR(8);  mid(1); kstep(TT{}, 1, 30, 0, ns);
        kstep(TT{}, 0, 30, 1, 0); WAIT_BAR(16); kstep(TT{}, 1, 31, 0, ns);
        kstep(TT{}, 0, 31, 1, 0); kstep(TT{}, 1, 31, 0, ns);
    } else {
        mid(0);
#pragma unroll
        for (int m = 0; m < 4; ++m) { acc[m][0] = f32x16{}; acc[m][1] = f32x16{}; }
#pragma unroll 1
        for (int t = 16; t < ns; ++t) {
            kstep(FF{}, 0, t, 1, 0);
            if (t < 19) { WAIT_BAR(24); } else if (t + 3 < ns) { WAIT_BAR(8); } else if (t + 2 < ns) { WAIT_BAR(4); } else { WAIT_BAR(0); }
            kstep(FF{}, 1, t + 1, 0, t + 4);
        }
    }
}

struct KvUnit {
    bf16x8 xf[8];
    DI void issue(int part, LAS char* lds, const P1& p, int tb, int isv, int wid, int lane) {
        const char* wsrc = (const char*)(isv ? p.wvA : p.wkA) + wid * 8192;
        glds16x4(wsrc + part * 4096, lane * 16, lds + part * 32768 + wid * 4096);
        if (part) {
#pragma unroll
            for (int s = 0; s < 8; ++s) xf[s] = *(const bf16x8*)((const char*)p.xA + (size_t)(32 * tb + (lane & 31)) * 2048 + (128 * wid + 16 * s + 8 * (lane >> 5)) * 2);
        }
    }
    DI void finish(LAS char* lds, const P1& p, int tb, int isv, int wid, int lane) {
        const int h = lane >> 5, blk = (lane >> 4) & 1, q = (lane & 15) >> 2, pq = lane & 3;
        asm volatile("s_waitcnt vmcnt(16)" ::: "memory");
        f32x16 ak = f32x16{};
#pragma unroll
        for (int s = 0; s < 8; ++s) {
            LAS const char* b0 = lds + (s >> 2) * 32768 + wid * 4096 + (16 * (s & 3) + 8 * h + q) * 64 + 32 * blk + 8 * pq;
            const bf16x8 wf = cat8(lds_tr(b0), lds_tr(b0 + 256));
            if (isv) ak = mfma32(xf[s], wf, ak); else ak = mfma32(wf, xf[s], ak);
        }
        asm volatile("s_waitcnt lgkmcnt(0)" ::: "memory");
#pragma unroll
        for (int i = 0; i < 4; ++i) *(LAS f32x4*)(lds + wid * 4096 + i * 1024 + lane * 16) = (f32x4){ak[4 * i], ak[4 * i + 1], ak[4 * i + 2], ak[4 * i + 3]};
        asm volatile("s_waitcnt lgkmcnt(0)\n\ts_barrier" ::: "memory");
        if (wid < 2) {
            f32x4 s0 = {0.f, 0.f, 0.f, 0.f}, s1 = {0.f, 0.f, 0.f, 0.f};
#pragma unroll
            for (int w = 0; w < 8; ++w) { s0 += *(LAS const f32x4*)(lds + w * 4096 + (2 * wid) * 1024 + lane * 16); s1 += *(LAS const f32x4*)(lds + w * 4096 + (2 * wid + 1) * 1024 + lane * 16); }
            u32x4 o; o.x = cvtpk(s0[0], s0[1]); o.y = cvtpk(s0[2], s0[3]); o.z = cvtpk(s1[0], s1[1]); o.w = cvtpk(s1[2], s1[3]);
            *(u32x4*)(p.ws + (isv ? WS_VA : WS_KA) + (size_t)(tb * 2 + wid) * 1024 + lane * 16) = o;
        }
    }
};

__global__ __launch_bounds__(512, 2) void k_proj(P1 p) {
    extern __shared__ __attribute__((aligned(16))) char smem[];
    LAS char* lds = (LAS char*)smem;
    const int tid = threadIdx.x, wid = __builtin_amdgcn_readfirstlane(tid >> 6), lane = tid & 63;
    const int bid = blockIdx.x, xcd = bid & 7, idx = bid >> 3;
    STAMP(p.ws, 0, 0);
    if (bid == 0 && tid < 128) ((unsigned*)(p.ws + WS_FLAG))[tid * 16] = 0u;
    const int wr = wid >> 2, wc = wid & 3;
    const int pm = idx & 7, pn = 2 * xcd + ((idx >> 3) & 1), kind = idx >> 4;
    const int gw = bid * 8 + wid;
    const char* warm = gw < 1365 ? (const char*)p.xA + (size_t)gw * 3072 : (const char*)p.wqA + (size_t)(gw - 1365 < 681 ? gw - 1365 : 681) * 3072;
    f32x16 acc[4][2];
    auto store_f = [&](char* base, float sc) {
#pragma unroll
        for (int m = 0; m < 4; ++m)
#pragma unroll
            for (int n = 0; n < 2; ++n) { const int tb = pm * 8 + wr * 4 + m, fb = pn * 8 + wc * 2 + n, hd = fb >> 2, db = fb & 3;
                char* dst = base + (size_t)((hd * NTB + tb) * 8 + 2 * db) * 1024 + lane * 16;
                st16(dst, pack8(acc[m][n], 0, sc)); st16(dst + 1024, pack8(acc[m][n], 8, sc)); }
    };
    if (kind == 0) {
        KvUnit kv; const int tbk = xcd * 8 + pm, isv = (idx >> 3) & 1;
        big_stream<false>(lds, (const char*)p.xB + (size_t)pm * 256 * 2048, 2048, (const char*)p.wqB + pn * 512, nullptr, acc,
                          [&](int part) { kv.issue(part, lds, p, tbk, isv, wid, lane); }, warm, (const char*)(isv ? p.wvA : p.wkA) + wid * 8192, wid, lane);
        SBAR();
        store_f(p.ws + WS_QB, CB);
        SBAR();
        kv.finish(lds, p, tbk, isv, wid, lane);
    } else {
        big_stream<true>(lds, (const char*)p.cKV + (size_t)pm * 256 * 1024, 1024, (const char*)p.wukB + pn * 512, (const char*)p.wuvB + pn * 512, acc,
                         [&](int) { store_f(p.ws + WS_KB, 1.f); }, warm, (const char*)p.wkA + wid * 8192, wid, lane);
#pragma unroll
        for (int m = 0; m < 4; ++m)
#pragma unroll
            for (int n = 0; n < 2; ++n) { const int tb = pm * 8 + wr * 4 + m, fb = pn * 8 + wc * 2 + n, hd = fb >> 2, db = fb & 3;
                char* dst = p.ws + WS_VB + (size_t)((hd * NTB + tb) * 8 + db) * 1024 + lane * 16;
                st16(dst, pack8(acc[m][n], 0, 1.f)); st16(dst + 4096, pack8(acc[m][n], 8, 1.f)); }
    }
    STAMP(p.ws, 0, 1); STAMP(p.ws, 0, 2); STAMP(p.ws, 0, 3);
}

struct P2 { const u16 *xA, *wqA, *woA, *woB; char* ws; };

DI void exp16(const f32x16& s, unsigned (&pk)[8], float& t0, float& t1, unsigned& orv) {
#pragma unroll
    for (int i = 0; i < 8; ++i) {
        const float a = __builtin_amdgcn_exp2f(s[2 * i]), b = __builtin_amdgcn_exp2f(s[2 * i + 1]);
        t0 += a; t1 += b; pk[i] = cvtpk(a, b); orv |= pk[i];
    }
}
DI float max16(const f32x16& s) {
    float m = fmaxf(s[0], s[1]);
#pragma unroll
    for (int i = 2; i < 16; ++i) m = fmaxf(m, s[i]);
    return m;
}
DI void diag_mask(f32x16& s, int r, int h) {
#pragma unroll
    for (int i = 0; i < 16; ++i) { const int kr = (i & 3) + 8 * (i >> 2) + 4 * h; if (kr > r) s[i] = -__builtin_inff(); }
}
DI bf16x8 pfrag(const unsigned (&pk)[8], int b) { u32x4 w = {pk[b], pk[b + 1], pk[b + 2], pk[b + 3]}; return __builtin_bit_cast(bf16x8, w); }

#define XEXP(x) __builtin_amdgcn_exp2f(x)
DI void attnB(LAS char* lds, char* ws, int hd, int xb, int wid, int lane) {
    const int r = lane & 31, h = lane >> 5;
    const int tbq = 4 * xb + wid, NT = 2 * (xb + 1);
    const char* kbase = ws + WS_KB + (size_t)hd * NTB * 8192 + wid * 4096;
    const char* vbase = ws + WS_VB + (size_t)hd * NTB * 8192 + wid * 4096;
    const unsigned lds0 = (unsigned)(size_t)lds + wid * 4096, lane16 = lane * 16;
    auto piece = [&](const char* src, unsigned m0v, int i) { if (i == 0) glds16o<0>(src, lane16, m0v); else if (i == 1) glds16o<1024>(src, lane16, m0v); else if (i == 2) glds16o<2048>(src, lane16, m0v); else glds16o<3072>(src, lane16, m0v); };
    glds16x4(kbase, lane * 16, lds + wid * 4096); glds16x4(vbase, lane * 16, lds + 32768 + wid * 4096); glds16x4(kbase + 16384, lane * 16, lds + 16384 + wid * 4096);
    bf16x8 qf[8];
#pragma unroll
    for (int s = 0; s < 8; ++s) qf[s] = *(const bf16x8*)(ws + WS_QB + (size_t)((hd * NTB + tbq) * 8 + s) * 1024 + lane * 16);
    f32x16 o[4], negm, S0, S1; float l = 0.f, t0, t1;
    bf16x8 fr[8]; unsigned pk[8], pn[4];
#pragma unroll
    for (int d = 0; d < 4; ++d) o[d] = f32x16{};
    LAS const char* lbase = lds + lane * 16;
    auto kaddr = [&](int j) { return lbase + ((j >> 1) & 1) * 16384 + (j & 1) * 8192; };
    auto vaddr = [&](int j) { return lbase + 32768 + ((j >> 1) & 1) * 16384 + (j & 1) * 8192; };
    auto slotA = [&](auto ST, f32x16& Sc, f32x16& Sn, int j, const char* src, unsigned m0v) {
        LAS const char* va = vaddr(j);
        float ea = 0.f, eb = 0.f;
#pragma unroll
        for (int i = 0; i < 4; ++i) pk[i] = pn[i];
#pragma unroll
        for (int i = 0; i < 8; ++i) {
            Sn = mfma32(fr[i], qf[i], i ? Sn : negm);
            fr[i] = lds_b128(va + i * 1024);
            if (!(i & 1)) { ea = XEXP(Sc[8 + i]); eb = XEXP(Sc[9 + i]); PIN(ea); PIN(eb); if (ST.value) piece(src, m0v, i >> 1); }
            else { pk[4 + (i >> 1)] = cvtpk(ea, eb); t0 += ea; t1 += eb; PIN(t0); PIN(t1); }
            SBAR();
        }
        float ts = t0 + t1;
        if (__builtin_expect(__any(!(ts < 1.f)), 0)) {
            const float delta = fmaxf(swap32_max(max16(Sc)) + SM_BIAS, 0.f), alpha = __builtin_amdgcn_exp2f(-delta);
            l *= alpha;
#pragma unroll
            for (int d = 0; d < 4; ++d) o[d] *= alpha;
            negm -= delta; Sn -= delta; t0 = 0.f; t1 = 0.f;
#pragma unroll
            for (int i = 0; i < 8; ++i) { const float a = __builtin_amdgcn_exp2f(Sc[2 * i] - delta), b = __builtin_amdgcn_exp2f(Sc[2 * i + 1] - delta); pk[i] = cvtpk(a, b); t0 += a; t1 += b; }
            ts = t0 + t1;
        }
        l += ts;
        if (j + 1 == tbq) diag_mask(Sn, r, h);
    };
    auto slotB = [&](auto ST, f32x16& Sn, int j, const char* src, unsigned m0v) {
        LAS const char* ka = kaddr(j + 2);
        float ea = 0.f, eb = 0.f;
        t0 = 0.f; t1 = 0.f;
#pragma unroll
        for (int i = 0; i < 8; ++i) {
            o[i & 3] = mfma32(fr[i], pfrag(pk, 4 * (i >> 2)), o[i & 3]);
            fr[i] = lds_b128(ka + i * 1024);
            if (!(i & 1)) { if (ST.value) piece(src, m0v, i >> 1); ea = XEXP(Sn[i]); eb = XEXP(Sn[i + 1]); PIN(ea); PIN(eb); }
            else { pn[i >> 1] = cvtpk(ea, eb); t0 += ea; t1 += eb; PIN(pn[i >> 1]); PIN(t0); PIN(t1); }
            SBAR();
        }
    };
    using TT = std::integral_constant<bool, true>; using FF = std::integral_constant<bool, false>;
    WAIT_BAR(0);
    {
        S0 = f32x16{};
#pragma unroll
        for (int s = 0; s < 8; ++s) fr[s] = lds_b128(lbase + s * 1024);
#pragma unroll
        for (int s = 0; s < 8; ++s) S0 = mfma32(fr[s], qf[s], S0);
#pragma unroll
        for (int s = 0; s < 8; ++s) fr[s] = lds_b128(lbase + 8192 + s * 1024);
        if (tbq == 0) diag_mask(S0, r, h);
        const float nm = -(swap32_max(max16(S0)) + SM_BIAS);
#pragma unroll
        for (int i = 0; i < 16; ++i) negm[i] = nm;
        S0 += nm;
        t0 = 0.f; t1 = 0.f;
#pragma unroll
        for (int i = 0; i < 4; ++i) { const float a = __builtin_amdgcn_exp2f(S0[2 * i]), b = __builtin_amdgcn_exp2f(S0[2 * i + 1]); pn[i] = cvtpk(a, b); t0 += a; t1 += b; }
    }
    for (int j = 0;; j += 2) {
        const int t = j >> 1;
        WAIT_BAR(0);
#define BPRIO_MASK 3
        if ((t & BPRIO_MASK) != BPRIO_MASK || BPRIO_MASK == 0) __builtin_amdgcn_s_setprio(2); else __builtin_amdgcn_s_setprio(0);
        const int tk = t + 2 < NT ? t + 2 : NT - 1, tv = t + 1 < NT ? t + 1 : NT - 1;
        slotA(TT{}, S0, S1, j, kbase + (size_t)tk * 16384, lds0 + (t & 1) * 16384);  slotB(TT{}, S1, j, vbase + (size_t)tv * 16384, lds0 + 32768 + ((t + 1) & 1) * 16384);
        if (j == tbq) break;
        slotA(FF{}, S1, S0, j + 1, nullptr, 0u); slotB(FF{}, S0, j + 1, nullptr, 0u);
        if (j + 1 == tbq) break;
    }
    if (wid < 2) WAIT_BAR(0);
    asm volatile("s_waitcnt vmcnt(0)" ::: "memory");
    const float inv = 1.f / swap32_add(l);
    char* dst = ws + WS_CTX + (size_t)(tbq * 256 + hd * 8) * 1024 + lane * 16;
#pragma unroll
    for (int d = 0; d < 4; ++d) { st16(dst + (2 * d) * 1024, pack8(o[d], 0, inv)); st16(dst + (2 * d + 1) * 1024, pack8(o[d], 8, inv)); }
}

DI void projA(LAS char* lds, const P2& p, int hg, int tg, bf16x8 (&qf)[2][2], int wid, int lane) {
    constexpr int STG = 24576, NST = 16;
    const int r = lane & 31, h = lane >> 5;
    const char* xsrc = (const char*)p.xA + (size_t)tg * 64 * 2048;
    unsigned voX[2], voW[4];
#pragma unroll
    for (int i = 0; i < 2; ++i) { const int row = (wid * 2 + i) * 8 + (lane >> 3), ch = (lane & 7) ^ ((row >> 1) & 7); voX[i] = (unsigned)row * 2048u + ch * 16; }
#pragma unroll
    for (int i = 0; i < 4; ++i) { const int k = ((wid & 1) * 4 + i) * 8 + (lane >> 3), slot = lane & 7;
        voW[i] = (unsigned)k * 2048u + (128 * hg + 64 * (wid >> 1) + 32 * ((slot >> 2) ^ ((k >> 1) & 1)) + 8 * (slot & 3)) * 2; }
    auto stage1 = [&](int slot, int t, int i) {
        LAS char* base = lds + slot * STG;
        if (i < 2) glds16(xsrc + (size_t)t * 128, voX[i], base + (wid * 2 + i) * 1024);
        else glds16((const char*)p.wqA + (size_t)t * 64 * 2048, voW[i - 2], base + 8192 + (wid >> 1) * 8192 + ((wid & 1) * 4 + i - 2) * 1024);
    };
    int aoff[4];
#pragma unroll
    for (int s = 0; s < 4; ++s) aoff[s] = r * 128 + (((2 * s + h) ^ ((r >> 1) & 7)) << 4);
    const int blk = (lane >> 4) & 1, q = (lane & 15) >> 2, pq = lane & 3;
    const int boff = 8192 + (wid >> 1) * 8192 + (8 * h + q) * 128 + 64 * ((wid & 1) ^ (q >> 1)) + 32 * blk + 8 * pq;
    f32x16 acc[2]; acc[0] = f32x16{}; acc[1] = f32x16{};
    bf16x8 wf[4], a0[4], a1[4];
    auto rd = [&](int slot, int s) { LAS const char* sa = lds + slot * STG;
        wf[s] = cat8(lds_tr(sa + boff + s * 2048), lds_tr(sa + boff + s * 2048 + 512)); a0[s] = lds_b128(sa + aoff[s]); a1[s] = lds_b128(sa + aoff[s] + 4096); };
#pragma unroll
    for (int t = 0; t < 3; ++t)
#pragma unroll
        for (int i = 0; i < 6; ++i) stage1(t, t, i);
    WAIT_BAR(12);
    rd(0, 0); rd(0, 1);
    int slot = 0;
    for (int t = 0; t < NST; ++t) {
        int sn = slot + 1; if (sn == 3) sn = 0;
        rd(slot, 2); acc[0] = mfma32(wf[0], a0[0], acc[0]); acc[1] = mfma32(wf[0], a1[0], acc[1]); SBAR();
        rd(slot, 3); acc[0] = mfma32(wf[1], a0[1], acc[0]); acc[1] = mfma32(wf[1], a1[1], acc[1]); SBAR();
        if (t + 2 < NST) { WAIT_BAR(6); } else { WAIT_BAR(0); }
        rd(sn, 0); acc[0] = mfma32(wf[2], a0[2], acc[0]); acc[1] = mfma32(wf[2], a1[2], acc[1]); SBAR();
        if (t + 3 < NST) { stage1(slot, t + 3, 0); stage1(slot, t + 3, 1); stage1(slot, t + 3, 2); }
        rd(sn, 1); acc[0] = mfma32(wf[3], a0[3], acc[0]); acc[1] = mfma32(wf[3], a1[3], acc[1]); SBAR();
        if (t + 3 < NST) { stage1(slot, t + 3, 3); stage1(slot, t + 3, 4); stage1(slot, t + 3, 5); }
        slot = sn;
    }
#pragma unroll
    for (int u = 0; u < 2; ++u)
#pragma unroll
        for (int s = 0; s < 2; ++s) qf[u][s] = __builtin_bit_cast(bf16x8, pack8(acc[u], 8 * s, CA));
    WAIT_BAR(0);
}
DI void attnA(LAS char* lds, const P2& p, int hg, int tg, int wid, int lane) {
    char* ws = p.ws;
    const int hd = 4 * hg + wid, tb0 = 2 * tg;
    bf16x8 qf[2][2];
    __builtin_amdgcn_s_setprio(3);
    projA(lds, p, hg, tg, qf, wid, lane);
    STAMP(ws, 1, 1);
    __builtin_amdgcn_s_setprio(1);
    STAMP(ws, 1, 2);
    const char* kvsrc = ws + ((wid & 2) ? WS_VA : WS_KA) + (wid & 1) * 4096;
    auto stage = [&](int slot, int t) { glds16x4(kvsrc + (size_t)t * 8192, lane * 16, lds + slot * 16384 + wid * 4096); };
    stage(0, 0); stage(1, 1);
    f32x16 o[2], negm[2], Sa[2], Sb[2]; float l[2] = {0.f, 0.f};
    o[0] = f32x16{}; o[1] = f32x16{};
    bf16x8 kf[2], vf[2]; unsigned pp[2][8];
    LAS const char* lbase = lds + lane * 16;
    auto step = [&](f32x16 (&Sc)[2], f32x16 (&Sn)[2], LAS const char* ka, LAS const char* va) {
        unsigned pn[2][8]; float t[2][2] = {{0.f, 0.f}, {0.f, 0.f}}, e[2][4];
#pragma unroll
        for (int i = 0; i < 9; ++i) {
            if (i < 8) {
                const int u = i & 1, s = (i >> 1) & 1;
                if (i < 4) Sn[u] = mfma32(kf[s], qf[u][s], s ? Sn[u] : negm[u]);
                else o[u] = mfma32(vf[s], pfrag(pp[u], 4 * s), o[u]);
                if (i == 3) { kf[0] = lds_b128(ka); kf[1] = lds_b128(ka + 1024); }
                if (i == 7) { vf[0] = lds_b128(va); vf[1] = lds_b128(va + 1024); }
#pragma unroll
                for (int k = 0; k < 4; ++k) { e[i & 1][k] = __builtin_amdgcn_exp2f(Sc[i >> 2][(i & 3) * 4 + k]); PIN(e[i & 1][k]); }
            }
            if (i > 0) {
                const int g = i - 1, uu = g >> 2, w = (g & 3) * 2; const float* x = e[g & 1];
                pn[uu][w] = cvtpk(x[0], x[1]); pn[uu][w + 1] = cvtpk(x[2], x[3]);
                t[uu][0] += x[0]; t[uu][1] += x[1]; t[uu][0] += x[2]; t[uu][1] += x[3]; PIN(t[uu][0]); PIN(t[uu][1]); PIN(pn[uu][w]); PIN(pn[uu][w + 1]);
            }
            if (i < 8) SBAR();
        }
        float ts[2] = {t[0][0] + t[0][1], t[1][0] + t[1][1]};
        if (__builtin_expect(__any(!(ts[0] + ts[1] < 1.f)), 0)) {
#pragma unroll
            for (int u = 0; u < 2; ++u) {
                const float dl = fmaxf(swap32_max(max16(Sc[u])) + SM_BIAS, 0.f), al = __builtin_amdgcn_exp2f(-dl);
                l[u] *= al; o[u] *= al; negm[u] -= dl; Sn[u] -= dl; float a0 = 0.f, a1 = 0.f;
#pragma unroll
                for (int i = 0; i < 8; ++i) { const float a = __builtin_amdgcn_exp2f(Sc[u][2 * i] - dl), b = __builtin_amdgcn_exp2f(Sc[u][2 * i + 1] - dl); pn[u][i] = cvtpk(a, b); a0 += a; a1 += b; }
                ts[u] = a0 + a1;
            }
        }
#pragma unroll
        for (int u = 0; u < 2; ++u) { l[u] += ts[u];
#pragma unroll
            for (int i = 0; i < 8; ++i) pp[u][i] = pn[u][i]; }
    };
    WAIT_BAR(4);
    {
        kf[0] = lds_b128(lbase); kf[1] = lds_b128(lbase + 1024);
#pragma unroll
        for (int u = 0; u < 2; ++u) {
            Sa[u] = mfma32(kf[0], qf[u][0], f32x16{}); Sa[u] = mfma32(kf[1], qf[u][1], Sa[u]);
            const float nm = -(swap32_max(max16(Sa[u])) + SM_BIAS);
#pragma unroll
            for (int i = 0; i < 16; ++i) negm[u][i] = nm;
            Sa[u] += nm;
#pragma unroll
            for (int i = 0; i < 8; ++i) pp[u][i] = 0u;
        }
        kf[0] = lds_b128(lbase + 2048); kf[1] = lds_b128(lbase + 2048 + 1024);
        vf[0] = kf[0]; vf[1] = kf[1];
    }
    int slot = 0;
    for (int t = 0; t < 16; ++t) {
        WAIT_BAR(0);
#define APRIO_T 99
        if (t == APRIO_T) __builtin_amdgcn_s_setprio(3);
        int sn = slot + 1; if (sn == 3) sn = 0; int sp = sn + 1; if (sp == 3) sp = 0;
        if (t + 2 < 16) stage(sp, t + 2);
        if (t == 2) { const int gw = (hg * 32 + tg) * 4 + wid; l2pf((const char*)p.woB + (size_t)gw * 8192, lane * 128, lds + 49152 + wid * 256);
                      l2pf((const char*)p.woA + (size_t)gw * 2048, (lane & 15) * 128, lds + 49152 + wid * 256); }
        LAS const char* cur = lbase + slot * 16384; LAS const char* nxt = lbase + sn * 16384;
        step(Sa, Sb, cur + 2 * 2048, cur + 8192);
        step(Sb, Sa, cur + 3 * 2048, cur + 8192 + 2048);
        step(Sa, Sb, nxt, cur + 8192 + 2 * 2048);
        step(Sb, Sa, nxt + 2048, cur + 8192 + 3 * 2048);
        slot = sn;
    }
#pragma unroll
    for (int s = 0; s < 2; ++s)
#pragma unroll
        for (int u = 0; u < 2; ++u) o[u] = mfma32(vf[s], pfrag(pp[u], 4 * s), o[u]);
#pragma unroll
    for (int u = 0; u < 2; ++u) {
        const float inv = 1.f / swap32_add(l[u]);
        char* dst = ws + WS_OA + (size_t)((tb0 + u) * 64 + hd * 2) * 1024 + lane * 16;
        st16(dst, pack8(o[u], 0, inv)); st16(dst + 1024, pack8(o[u], 8, inv));
    }
}

__global__ __launch_bounds__(256, 2) void k_attn(P2 p) {
    extern __shared__ __attribute__((aligned(16))) char smem[];
    LAS char* lds = (LAS char*)smem;
    const int tid = threadIdx.x, wid = __builtin_amdgcn_readfirstlane(tid >> 6), lane = tid & 63;
    const int bid = blockIdx.x;
    const int xcd = bid & 7, idx = bid >> 3,
        kind = (idx ^ (idx >> 5)) & 1,
        it = (idx >> 1) & 15 | (idx >> 5) << 4;
    STAMP(p.ws, 1, 0);
    if (kind == 0) { attnA(lds, p, xcd, it, wid, lane); }
    else {
        const int hd = xcd + 8 * (it >> 3), x = it & 7;
        attnB(lds, p.ws, hd, 15 - x, wid, lane);
        WAIT_BAR(0);
        STAMP(p.ws, 1, 1);
        attnB(lds, p.ws, hd, x, wid, lane); STAMP(p.ws, 1, 2); }
    STAMP(p.ws, 1, 3);
}

struct P3 { const u16 *woA, *woB; char* ws; u16* out; };

constexpr int NT1 = 24;
DI void out_store(u16* out, int tb0, int col0, const f32x16 (&acc)[2][2], int wid, int lane) {
    const int wm = wid >> 1, wn = wid & 1, r = lane & 31, h = lane >> 5;
#pragma unroll
    for (int m = 0; m < 2; ++m) {
        char* dst = (char*)out + (size_t)((tb0 + 2 * wm + m) * 32 + r) * 4096 + (col0 + 64 * wn + 8 * h) * 2;
#pragma unroll
        for (int c = 0; c < 2; ++c) { *(u32x4*)(dst + c * 64) = pack8(acc[m][c], 0, 1.f); *(u32x4*)(dst + c * 64 + 32) = pack8(acc[m][c], 8, 1.f); }
    }
}

__global__ __launch_bounds__(512, 2) void k_out(P3 p) {
    extern __shared__ __attribute__((aligned(16))) char smem[];
    LAS char* lds = (LAS char*)smem;
    constexpr int STG = 32768, nt = 40;
    const int tid = threadIdx.x, wid = __builtin_amdgcn_readfirstlane(tid >> 6), lane = tid & 63;
    const int bid = blockIdx.x, xg = bid & 7, role = (xg >> 2) ^ 1, ti = bid >> 3;
    const int pm = 8 * ((xg >> 1) & 1) + (ti >> 2), pn = 4 * (xg & 1) + (ti & 3), tb0 = pm * 4, tau = pm * 8 + pn;
    STAMP(p.ws, 2, 0);
    if (wid >= 4) {
        const int lw = wid - 4;
        const char* a0 = p.ws + WS_CTX + (size_t)((tb0 + lw) * 256 + (role ? 4 * (64 - NT1) : 0)) * 1024;
        const char* w0 = (const char*)p.woB + pn * 256 + (size_t)(role ? 64 - NT1 : 0) * 64 * 2048;
        const char* a1 = p.ws + WS_OA + (size_t)((tb0 + lw) * 64) * 1024 - (size_t)NT1 * 4096;
        const char* w1 = (const char*)p.woA + pn * 256 - (size_t)NT1 * 64 * 2048;
        const int tsw = role ? NT1 : nt;
        unsigned voW[4];
#pragma unroll
        for (int i = 0; i < 4; ++i) { const int pc = 4 * lw + i, g = pc >> 3, k = (pc & 7) * 8 + (lane >> 3), slot = lane & 7;
            const int col = 64 * g + 32 * ((slot >> 2) ^ ((k >> 1) & 1)) + 8 * (slot & 3); voW[i] = (unsigned)k * 2048u + col * 2; }
        auto stage = [&](int t) {
            LAS char* base = lds + (t & 3) * STG;
            const char* as = (t < tsw ? a0 : a1) + (size_t)t * 4096; const char* wsd = (t < tsw ? w0 : w1) + (size_t)t * (64 * 2048);
            glds16x4(as, lane * 16, base + lw * 4096);
#pragma unroll
            for (int i = 0; i < 4; ++i) glds16(wsd, voW[i], base + 16384 + (4 * lw + i) * 1024);
        };
#pragma unroll
        for (int t = 0; t < 4; ++t) stage(t);
        WAIT_BAR(24);
        for (int t = 0; t < nt; ++t) {
            if (t + 3 < nt) { WAIT_BAR(16); } else if (t + 2 < nt) { WAIT_BAR(8); } else { WAIT_BAR(0); }
            if (t + 4 < nt) stage(t + 4);
        }
    } else {
        const int wm = wid >> 1, wn = wid & 1, h = lane >> 5;
        const int blk = (lane >> 4) & 1, q = (lane & 15) >> 2, pq = lane & 3, pp = ((pq & 1) << 1) | (pq >> 1);
        GAS unsigned* flag = (GAS unsigned*)(p.ws + WS_FLAG) + tau * 16;
        const __amdgpu_buffer_rsrc_t rs = __builtin_amdgcn_make_buffer_rsrc(p.ws + WS_PART + (size_t)tau * 65536, 0, 65536, 0x00020000);
        int boff[2];
#pragma unroll
        for (int c = 0; c < 2; ++c) boff[c] = 16384 + wn * 8192 + (4 * h + q) * 128 + 64 * (c ^ (q >> 1)) + 32 * blk + 8 * pp;
        const int aoff = (2 * wm * 4) * 1024 + lane * 16;
        f32x16 acc[2][2];
#pragma unroll
        for (int m = 0; m < 2; ++m) { acc[m][0] = f32x16{}; acc[m][1] = f32x16{}; }
        bf16x8 af[4][2], wf[4][2];
        auto rd = [&](int slot, int s, int i) {
            LAS const char* sa = lds + slot * STG;
            if (i & 1) af[s][i >> 1] = lds_b128(sa + aoff + ((i >> 1) * 4 + s) * 1024);
            else wf[s][i >> 1] = cat8(lds_tr(sa + boff[i >> 1] + s * 2048), lds_tr(sa + boff[i >> 1] + s * 2048 + 1024));
        };
        auto kstep = [&](int s, int nslot) {
#pragma unroll
            for (int i = 0; i < 4; ++i) {
                const int m = i >> 1, c = i & 1;
                acc[m][c] = mfma32(wf[s][c], af[s][m], acc[m][c]);
                rd(nslot, (s + 2) & 3, i);
                SBAR();
            }
        };
        auto tiles = [&](int t0, int t1) {
#pragma unroll 1
            for (int t = t0; t < t1; ++t) {
                kstep(0, t & 3); kstep(1, t & 3);
                asm volatile("s_waitcnt lgkmcnt(0)\n\ts_barrier" ::: "memory");
                kstep(2, (t + 1) & 3); kstep(3, (t + 1) & 3);
            }
        };
        WAIT_BAR(0);
#pragma unroll
        for (int i = 0; i < 4; ++i) { rd(0, 0, i); rd(0, 1, i); }
        if (role == 1) {
            tiles(0, NT1);
#pragma unroll
            for (int m = 0; m < 2; ++m)
#pragma unroll
                for (int c = 0; c < 2; ++c) {
#pragma unroll
                    for (int i = 0; i < 4; ++i) { u32x4 v;
#pragma unroll
                        for (int j = 0; j < 4; ++j) v[j] = __float_as_uint(acc[m][c][4 * i + j]);
                        __builtin_amdgcn_raw_buffer_store_b128(v, rs, wid * 16384 + ((m * 2 + c) * 4 + i) * 1024 + lane * 16, 0, 16); }
                    acc[m][c] = f32x16{}; }
            tiles(NT1, NT1 + 4);
            asm volatile("s_waitcnt vmcnt(0)" ::: "memory");
            tiles(NT1 + 4, NT1 + 5);
            if (tid == 0) __hip_atomic_store(flag, 1u, __ATOMIC_RELAXED, __HIP_MEMORY_SCOPE_AGENT);
            STAMP(p.ws, 2, 1);
            tiles(NT1 + 5, nt);
            STAMP(p.ws, 2, 2);
            out_store(p.out, tb0, pn * 128, acc, wid, lane);
        } else {
            tiles(0, nt - 5);
            while ((unsigned)__builtin_amdgcn_readfirstlane(__hip_atomic_load(flag, __ATOMIC_RELAXED, __HIP_MEMORY_SCOPE_AGENT)) != 1u) __builtin_amdgcn_s_sleep(2);
            __builtin_amdgcn_fence(__ATOMIC_ACQUIRE, "wavefront");
            u32x4 pv[16];
#pragma unroll
            for (int i = 0; i < 16; ++i) pv[i] = __builtin_amdgcn_raw_buffer_load_b128(rs, wid * 16384 + i * 1024 + lane * 16, 0, 16);
            STAMP(p.ws, 2, 1);
            tiles(nt - 5, nt);
#pragma unroll
            for (int i = 0; i < 16; ++i)
#pragma unroll
                for (int j = 0; j < 4; ++j) acc[i >> 3][(i >> 2) & 1][4 * (i & 3) + j] += __uint_as_float(pv[i][j]);
            STAMP(p.ws, 2, 2);
            out_store(p.out, tb0, 1024 + pn * 128, acc, wid, lane);
        }
        STAMP(p.ws, 2, 3);
    }
}

#define BENCH_MARK(s)
extern "C" void kernel_launch(void* const* d_in, const int* in_sizes, int n_in, void* d_out, int out_size, void* d_ws, size_t ws_size,
                              hipStream_t stream) {
    (void)in_sizes; (void)n_in; (void)out_size; (void)ws_size;
    (void)hipFuncSetAttribute((const void*)k_proj, hipFuncAttributeMaxDynamicSharedMemorySize, 133120);
    (void)hipFuncSetAttribute((const void*)k_attn, hipFuncAttributeMaxDynamicSharedMemorySize, 73728);
    (void)hipFuncSetAttribute((const void*)k_out, hipFuncAttributeMaxDynamicSharedMemorySize, 131072);
    P1 p1; p1.wqA = (const u16*)d_in[1]; p1.xA = (const u16*)d_in[0]; p1.wkA = (const u16*)d_in[2]; p1.wvA = (const u16*)d_in[3];
    p1.xB = (const u16*)d_in[5]; p1.cKV = (const u16*)d_in[6]; p1.wqB = (const u16*)d_in[7]; p1.wukB = (const u16*)d_in[8]; p1.wuvB = (const u16*)d_in[9];
    p1.ws = (char*)d_ws;
    BENCH_MARK(stream);
    k_proj<<<256, 512, 133120, stream>>>(p1);
    BENCH_MARK(stream);
    P2 p2; p2.woA = (const u16*)d_in[4]; p2.woB = (const u16*)d_in[10]; p2.xA = (const u16*)d_in[0]; p2.wqA = (const u16*)d_in[1]; p2.ws = (char*)d_ws;
    k_attn<<<512, 256, 73728, stream>>>(p2);
    BENCH_MARK(stream);
    P3 p3; p3.woA = (const u16*)d_in[4]; p3.woB = (const u16*)d_in[10]; p3.ws = (char*)d_ws; p3.out = (u16*)d_out;
    k_out<<<256, 512, 131072, stream>>>(p3);
    BENCH_MARK(stream);
}
```

```cpp
#include <hip/hip_runtime.h>
#include <hip/hip_bf16.h>
#include <type_traits>

typedef unsigned short u16;
typedef short bf16x8 __attribute__((ext_vector_type(8)));
typedef short bf16x4 __attribute__((ext_vector_type(4)));
typedef float f32x16 __attribute__((ext_vector_type(16)));
typedef float f32x4 __attribute__((ext_vector_type(4)));
typedef float f32x2 __attribute__((ext_vector_type(2)));
typedef __bf16 bf16x2_t __attribute__((ext_vector_type(2)));
typedef unsigned u32x4 __attribute__((ext_vector_type(4)));
#define LAS __attribute__((address_space(3)))
#define GAS __attribute__((address_space(1)))
#define DI __device__ __forceinline__

DI f32x16 mfma32(bf16x8 a, bf16x8 b, f32x16 c) { return __builtin_amdgcn_mfma_f32_32x32x16_bf16(a, b, c, 0, 0, 0); }
DI unsigned cvtpk(float lo, float hi) { f32x2 v = {lo, hi}; bf16x2_t b = __builtin_convertvector(v, bf16x2_t); return __builtin_bit_cast(unsigned, b); }
DI void glds16(const void* sbase, unsigned voff, LAS void* l) {
    unsigned sv; asm volatile("s_mov_b32 %0, m0\n\ts_mov_b32 m0, %3\n\ts_nop 0\n\tglobal_load_lds_dwordx4 %1, %2\n\ts_mov_b32 m0, %0"
                              : "=&s"(sv) : "v"(voff), "s"(sbase), "s"((unsigned)(size_t)l) : "memory"); }
template <int OFF> DI void glds16o(const void* sbase, unsigned voff, unsigned m0v) {
    asm volatile("s_mov_b32 m0, %2\n\ts_nop 0\n\tglobal_load_lds_dwordx4 %0, %1 offset:%3" :: "v"(voff), "s"(sbase), "s"(m0v), "i"(OFF) : "memory"); }
DI void l2pf(const void* sbase, unsigned voff, LAS void* scratch) {
    unsigned sv; asm volatile("s_mov_b32 %0, m0\n\ts_mov_b32 m0, %3\n\ts_nop 0\n\tglobal_load_lds_dword %1, %2\n\ts_mov_b32 m0, %0"
                              : "=&s"(sv) : "v"(voff), "s"(sbase), "s"((unsigned)(size_t)scratch) : "memory"); }
DI void l2pfv(const void* vaddr, LAS void* scratch) {
    unsigned sv; asm volatile("s_mov_b32 %0, m0\n\ts_mov_b32 m0, %2\n\ts_nop 0\n\tglobal_load_lds_dword %1, off\n\ts_mov_b32 m0, %0"
                              : "=&s"(sv) : "v"(vaddr), "s"((unsigned)(size_t)scratch) : "memory"); }
DI void glds16v(const void* vaddr, LAS void* l) {
    unsigned sv; asm volatile("s_mov_b32 %0, m0\n\ts_mov_b32 m0, %2\n\ts_nop 0\n\tglobal_load_lds_dwordx4 %1, off\n\ts_mov_b32 m0, %0"
                              : "=&s"(sv) : "v"(vaddr), "s"((unsigned)(size_t)l) : "memory"); }
DI void glds16x4(const void* sbase, unsigned voff, LAS void* l) {
    unsigned sv; asm volatile("s_mov_b32 %0, m0\n\ts_mov_b32 m0, %3\n\ts_nop 0\n\tglobal_load_lds_dwordx4 %1, %2\n\tglobal_load_lds_dwordx4 %1, %2 offset:1024\n\t"
                              "global_load_lds_dwordx4 %1, %2 offset:2048\n\tglobal_load_lds_dwordx4 %1, %2 offset:3072\n\ts_mov_b32 m0, %0"
                              : "=&s"(sv) : "v"(voff), "s"(sbase), "s"((unsigned)(size_t)l) : "memory"); }
DI void glds16x2(const void* sbase, unsigned voff, LAS void* l) {
    unsigned sv; asm volatile("s_mov_b32 %0, m0\n\ts_mov_b32 m0, %3\n\ts_nop 0\n\tglobal_load_lds_dwordx4 %1, %2\n\tglobal_load_lds_dwordx4 %1, %2 offset:1024\n\ts_mov_b32 m0, %0"
                              : "=&s"(sv) : "v"(voff), "s"(sbase), "s"((unsigned)(size_t)l) : "memory"); }
DI bf16x8 lds_b128(LAS const char* p) { return *(LAS const bf16x8*)p; }
DI bf16x4 lds_tr(LAS const char* p) { return __builtin_amdgcn_ds_read_tr16_b64_v4i16((LAS bf16x4*)p); }
DI bf16x8 cat8(bf16x4 a, bf16x4 b) { return (bf16x8){a[0], a[1], a[2], a[3], b[0], b[1], b[2], b[3]}; }
DI float swap32_max(float x) { auto rr = __builtin_amdgcn_permlane32_swap(__float_as_uint(x), __float_as_uint(x), false, false); return fmaxf(__uint_as_float(rr[0]), __uint_as_float(rr[1])); }
DI float swap32_add(float x) { auto rr = __builtin_amdgcn_permlane32_swap(__float_as_uint(x), __float_as_uint(x), false, false); return __uint_as_float(rr[0]) + __uint_as_float(rr[1]); }
#define WAIT_BAR(N) asm volatile("s_waitcnt vmcnt(" #N ") lgkmcnt(0)\n\ts_barrier" ::: "memory")
#define SBAR() __builtin_amdgcn_sched_barrier(0)
#define PIN(x) asm volatile("" : "+v"(x))
#define WAIT_BAR_I(N) asm volatile("s_waitcnt vmcnt(%0) lgkmcnt(0)\n\ts_barrier" :: "i"(N) : "memory")

DI void st16(void* p, u32x4 v) { *(u32x4*)p = v; }
DI u32x4 pack8(const f32x16& a, int b, float sc) {
    u32x4 w; w.x = cvtpk(a[b + 0] * sc, a[b + 1] * sc); w.y = cvtpk(a[b + 2] * sc, a[b + 3] * sc);
    w.z = cvtpk(a[b + 4] * sc, a[b + 5] * sc); w.w = cvtpk(a[b + 6] * sc, a[b + 7] * sc); return w;
}

constexpr int SEQ = 2048, NTB = SEQ / 32;
constexpr float LOG2E = 1.4426950408889634f;
constexpr float CA = 0.17677669529663687f * LOG2E;
constexpr float CB = 0.08838834764831845f * LOG2E;
#define SMB 64.f
constexpr float SM_BIAS = SMB;

constexpr size_t WS_QB = 0, WS_KB = WS_QB + (16u << 20), WS_VB = WS_KB + (16u << 20), WS_QA = WS_VB + (16u << 20),
                 WS_KA = WS_QA + (4u << 20), WS_VA = WS_KA + (128u << 10), WS_CTX = WS_VA + (128u << 10), WS_OA = WS_CTX + (16u << 20),
                 WS_PART = WS_OA + (4u << 20), WS_FLAG = WS_PART + (8u << 20), WS_END = WS_FLAG + 129 * 64;

#define STAMP(ws, kn, i)
struct P1 { const u16 *xB, *cKV, *wqB, *wukB, *wuvB, *xA, *wkA, *wvA, *wqA; char* ws; };

template <bool KV, class Mid>
DI void big_stream(LAS char* lds, const char* Ab, unsigned lda_b, const char* W0, const char* W1, f32x16 (&acc)[4][2], Mid mid, const char* warm, const char* warm2, int wid, int lane) {
    constexpr unsigned ldw_b = 8192; constexpr int ns = 32;
    const int wr = wid >> 2, wc = wid & 3, r = lane & 31, h = lane >> 5;
    unsigned voA[2], voW[2];
#pragma unroll
    for (int i = 0; i < 2; ++i) {
        const int row = (wid * 2 + i) * 16 + (lane >> 2), ch = (lane & 3) ^ ((row >> 2) & 3);
        voA[i] = (unsigned)row * lda_b + ch * 16;
        const int k = ((wid & 1) * 2 + i) * 8 + (lane >> 3), slot = lane & 7;
        const int col = 64 * (wid >> 1) + 32 * ((slot >> 2) ^ ((k >> 1) & 1)) + 8 * (slot & 3);
        voW[i] = (unsigned)k * ldw_b + col * 2;
    }
    int aoff[2], boff[2];
#pragma unroll
    for (int s = 0; s < 2; ++s) aoff[s] = (128 * wr + r) * 64 + (((2 * s + h) ^ ((r >> 2) & 3)) << 4);
    { const int blk = (lane >> 4) & 1, q = (lane & 15) >> 2, p = lane & 3;
#pragma unroll
      for (int n = 0; n < 2; ++n) boff[n] = 16384 + wc * 4096 + (8 * h + q) * 128 + 64 * (n ^ (q >> 1)) + 32 * blk + 8 * p; }
#pragma unroll
    for (int m = 0; m < 4; ++m) { acc[m][0] = f32x16{}; acc[m][1] = f32x16{}; }
    auto stage1 = [&](int t, int i) {
        LAS char* base = lds + (t & 3) * 32768;
        const int tk = KV ? (t & 15) : t;
        if (i < 2) glds16(Ab + (size_t)tk * 64, voA[i], base + (wid * 2 + i) * 1024);
        else glds16(((KV && t >= 16) ? W1 : W0) + (size_t)tk * 32 * ldw_b, voW[i - 2], base + 16384 + (wid >> 1) * 4096 + ((wid & 1) * 2 + i - 2) * 1024);
    };
    bf16x8 af[2][4], wf[2][2];
    auto rd = [&](int rs, int t, int s, int i) {
        LAS const char* sa = lds + (t & 3) * 32768;
        if (i < 2) wf[rs][i] = cat8(lds_tr(sa + boff[i] + s * 2048), lds_tr(sa + boff[i] + s * 2048 + 512));
        else af[rs][i - 2] = lds_b128(sa + aoff[s] + (i - 2) * 2048);
    };
    auto kstep = [&](auto OT, int rs, int nt_, int nstep, int st) {
#pragma unroll
        for (int i = 0; i < 8; ++i) {
            const int m = i >> 1, n = i & 1;
            acc[m][n] = OT.value ? mfma32(wf[rs][n], af[rs][m], acc[m][n]) : mfma32(af[rs][m], wf[rs][n], acc[m][n]);
            if (i < 6) rd(rs ^ 1, nt_, nstep, i);
            if (rs == 1 && (i & 1) && st < ns) stage1(st, i >> 1);
            SBAR();
        }
    };
    using TT = std::integral_constant<bool, true>; using FF = std::integral_constant<bool, false>;
#pragma unroll
    for (int t = 0; t < 4; ++t)
#pragma unroll
        for (int i = 0; i < 4; ++i) stage1(t, i);
    WAIT_BAR(12);
#pragma unroll
    for (int i = 0; i < 6; ++i) rd(0, 0, 0, i);
#pragma unroll 1
    for (int t = 0; t < 12; ++t) {
        kstep(TT{}, 0, t, 1, 0);
        WAIT_BAR(8);
        kstep(TT{}, 1, t + 1, 0, t + 4);
    }
    kstep(TT{}, 0, 12, 1, 0); WAIT_BAR(8);
    l2pf(warm, (lane < 24 ? lane : 0) * 128, lds + 131072 + wid * 256); l2pf(warm2, lane * 128, lds + 131072 + wid * 256);
    kstep(TT{}, 1, 13, 0, 16);
#pragma unroll 1
    for (int t = 13; t < 15; ++t) { kstep(TT{}, 0, t, 1, 0); WAIT_BAR(10); kstep(TT{}, 1, t + 1, 0, t + 4); }
#pragma unroll 1
    for (int t = 15; t < (KV ? 16 : 28); ++t) {
        kstep(TT{}, 0, t, 1, 0);
        WAIT_BAR(8);
        kstep(TT{}, 1, t + 1, 0, t + 4);
    }
    if constexpr (!KV) {
        kstep(TT{}, 0, 28, 1, 0); WAIT_BAR(8);  mid(0); kstep(TT{}, 1, 29, 0, ns);
        kstep(TT{}, 0, 29, 1, 0); WAIT_BAR(8);  mid(1); kstep(TT{}, 1, 30, 0, ns);
        kstep(TT{}, 0, 30, 1, 0); WAIT_BAR(16); kstep(TT{}, 1, 31, 0, ns);
        kstep(TT{}, 0, 31, 1, 0); kstep(TT{}, 1, 31, 0, ns);
    } else {
        mid(0);
#pragma unroll
        for (int m = 0; m < 4; ++m) { acc[m][0] = f32x16{}; acc[m][1] = f32x16{}; }
#pragma unroll 1
        for (int t = 16; t < ns; ++t) {
            kstep(FF{}, 0, t, 1, 0);
            if (t < 19) { WAIT_BAR(24); } else if (t + 3 < ns) { WAIT_BAR(8); } else if (t + 2 < ns) { WAIT_BAR(4); } else { WAIT_BAR(0); }
            kstep(FF{}, 1, t + 1, 0, t + 4);
        }
    }
}

struct KvUnit {
    bf16x8 xf[8];
    DI void issue(int part, LAS char* lds, const P1& p, int tb, int isv, int wid, int lane) {
        const char* wsrc = (const char*)(isv ? p.wvA : p.wkA) + wid * 8192;
        glds16x4(wsrc + part * 4096, lane * 16, lds + part * 32768 + wid * 4096);
        if (part) {
#pragma unroll
            for (int s = 0; s < 8; ++s) xf[s] = *(const bf16x8*)((const char*)p.xA + (size_t)(32 * tb + (lane & 31)) * 2048 + (128 * wid + 16 * s + 8 * (lane >> 5)) * 2);
        }
    }
    DI void finish(LAS char* lds, const P1& p, int tb, int isv, int wid, int lane) {
        const int h = lane >> 5, blk = (lane >> 4) & 1, q = (lane & 15) >> 2, pq = lane & 3;
        asm volatile("s_waitcnt vmcnt(16)" ::: "memory");
        f32x16 ak = f32x16{};
#pragma unroll
        for (int s = 0; s < 8; ++s) {
            LAS const char* b0 = lds + (s >> 2) * 32768 + wid * 4096 + (16 * (s & 3) + 8 * h + q) * 64 + 32 * blk + 8 * pq;
            const bf16x8 wf = cat8(lds_tr(b0), lds_tr(b0 + 256));
            if (isv) ak = mfma32(xf[s], wf, ak); else ak = mfma32(wf, xf[s], ak);
        }
        asm volatile("s_waitcnt lgkmcnt(0)" ::: "memory");
#pragma unroll
        for (int i = 0; i < 4; ++i) *(LAS f32x4*)(lds + wid * 4096 + i * 1024 + lane * 16) = (f32x4){ak[4 * i], ak[4 * i + 1], ak[4 * i + 2], ak[4 * i + 3]};
        asm volatile("s_waitcnt lgkmcnt(0)\n\ts_barrier" ::: "memory");
        if (wid < 2) {
            f32x4 s0 = {0.f, 0.f, 0.f, 0.f}, s1 = {0.f, 0.f, 0.f, 0.f};
#pragma unroll
            for (int w = 0; w < 8; ++w) { s0 += *(LAS const f32x4*)(lds + w * 4096 + (2 * wid) * 1024 + lane * 16); s1 += *(LAS const f32x4*)(lds + w * 4096 + (2 * wid + 1) * 1024 + lane * 16); }
            u32x4 o; o.x = cvtpk(s0[0], s0[1]); o.y = cvtpk(s0[2], s0[3]); o.z = cvtpk(s1[0], s1[1]); o.w = cvtpk(s1[2], s1[3]);
            *(u32x4*)(p.ws + (isv ? WS_VA : WS_KA) + (size_t)(tb * 2 + wid) * 1024 + lane * 16) = o;
        }
    }
};

__global__ __launch_bounds__(512, 2) void k_proj(P1 p) {
    extern __shared__ __attribute__((aligned(16))) char smem[];
    LAS char* lds = (LAS char*)smem;
    const int tid = threadIdx.x, wid = __builtin_amdgcn_readfirstlane(tid >> 6), lane = tid & 63;
    const int bid = blockIdx.x, xcd = bid & 7, idx = bid >> 3;
    STAMP(p.ws, 0, 0);
    if (bid == 0 && tid < 128) ((unsigned*)(p.ws + WS_FLAG))[tid * 16] = 0u;
    const int wr = wid >> 2, wc = wid & 3;
    const int pm = idx & 7, pn = 2 * xcd + ((idx >> 3) & 1), kind = idx >> 4;
    const int gw = bid * 8 + wid;
    const char* warm = gw < 1365 ? (const char*)p.xA + (size_t)gw * 3072 : (const char*)p.wqA + (size_t)(gw - 1365 < 681 ? gw - 1365 : 681) * 3072;
    f32x16 acc[4][2];
    auto store_f = [&](char* base, float sc) {
#pragma unroll
        for (int m = 0; m < 4; ++m)
#pragma unroll
            for (int n = 0; n < 2; ++n) { const int tb = pm * 8 + wr * 4 + m, fb = pn * 8 + wc * 2 + n, hd = fb >> 2, db = fb & 3;
                char* dst = base + (size_t)((hd * NTB + tb) * 8 + 2 * db) * 1024 + lane * 16;
                st16(dst, pack8(acc[m][n], 0, sc)); st16(dst + 1024, pack8(acc[m][n], 8, sc)); }
    };
    if (kind == 0) {
        KvUnit kv; const int tbk = xcd * 8 + pm, isv = (idx >> 3) & 1;
        big_stream<false>(lds, (const char*)p.xB + (size_t)pm * 256 * 2048, 2048, (const char*)p.wqB + pn * 512, nullptr, acc,
                          [&](int part) { kv.issue(part, lds, p, tbk, isv, wid, lane); }, warm, (const char*)(isv ? p.wvA : p.wkA) + wid * 8192, wid, lane);
        SBAR();
        store_f(p.ws + WS_QB, CB);
        SBAR();
        kv.finish(lds, p, tbk, isv, wid, lane);
    } else {
        big_stream<true>(lds, (const char*)p.cKV + (size_t)pm * 256 * 1024, 1024, (const char*)p.wukB + pn * 512, (const char*)p.wuvB + pn * 512, acc,
                         [&](int) { store_f(p.ws + WS_KB, 1.f); }, warm, (const char*)p.wkA + wid * 8192, wid, lane);
#pragma unroll
        for (int m = 0; m < 4; ++m)
#pragma unroll
            for (int n = 0; n < 2; ++n) { const int tb = pm * 8 + wr * 4 + m, fb = pn * 8 + wc * 2 + n, hd = fb >> 2, db = fb & 3;
                char* dst = p.ws + WS_VB + (size_t)((hd * NTB + tb) * 8 + db) * 1024 + lane * 16;
                st16(dst, pack8(acc[m][n], 0, 1.f)); st16(dst + 4096, pack8(acc[m][n], 8, 1.f)); }
    }
    STAMP(p.ws, 0, 1); STAMP(p.ws, 0, 2); STAMP(p.ws, 0, 3);
}

struct P2 { const u16 *xA, *wqA, *woA, *woB; char* ws; };

DI void exp16(const f32x16& s, unsigned (&pk)[8], float& t0, float& t1, unsigned& orv) {
#pragma unroll
    for (int i = 0; i < 8; ++i) {
        const float a = __builtin_amdgcn_exp2f(s[2 * i]), b = __builtin_amdgcn_exp2f(s[2 * i + 1]);
        t0 += a; t1 += b; pk[i] = cvtpk(a, b); orv |= pk[i];
    }
}
DI float max16(const f32x16& s) {
    float m = fmaxf(s[0], s[1]);
#pragma unroll
    for (int i = 2; i < 16; ++i) m = fmaxf(m, s[i]);
    return m;
}
DI void diag_mask(f32x16& s, int r, int h) {
#pragma unroll
    for (int i = 0; i < 16; ++i) { const int kr = (i & 3) + 8 * (i >> 2) + 4 * h; if (kr > r) s[i] = -__builtin_inff(); }
}
DI bf16x8 pfrag(const unsigned (&pk)[8], int b) { u32x4 w = {pk[b], pk[b + 1], pk[b + 2], pk[b + 3]}; return __builtin_bit_cast(bf16x8, w); }

#define XEXP(x) __builtin_amdgcn_exp2f(x)
DI void attnB(LAS char* lds, char* ws, int hd, int xb, int wid, int lane) {
    const int r = lane & 31, h = lane >> 5;
    const int tbq = 4 * xb + wid, NT = 2 * (xb + 1);
    const char* kbase = ws + WS_KB + (size_t)hd * NTB * 8192 + wid * 4096;
    const char* vbase = ws + WS_VB + (size_t)hd * NTB * 8192 + wid * 4096;
    const unsigned lds0 = (unsigned)(size_t)lds + wid * 4096, lane16 = lane * 16;
    auto piece = [&](const char* src, unsigned m0v, int i) { if (i == 0) glds16o<0>(src, lane16, m0v); else if (i == 1) glds16o<1024>(src, lane16, m0v); else if (i == 2) glds16o<2048>(src, lane16, m0v); else glds16o<3072>(src, lane16, m0v); };
    glds16x4(kbase, lane * 16, lds + wid * 4096); glds16x4(vbase, lane * 16, lds + 32768 + wid * 4096); glds16x4(kbase + 16384, lane * 16, lds + 16384 + wid * 4096);
    bf16x8 qf[8];
#pragma unroll
    for (int s = 0; s < 8; ++s) qf[s] = *(const bf16x8*)(ws + WS_QB + (size_t)((hd * NTB + tbq) * 8 + s) * 1024 + lane * 16);
    f32x16 o[4], negm, S0, S1; float l = 0.f, t0, t1;
    bf16x8 fr[8]; unsigned pk[8], pn[4];
#pragma unroll
    for (int d = 0; d < 4; ++d) o[d] = f32x16{};
    LAS const char* lbase = lds + lane * 16;
    auto kaddr = [&](int j) { return lbase + ((j >> 1) & 1) * 16384 + (j & 1) * 8192; };
    auto vaddr = [&](int j) { return lbase + 32768 + ((j >> 1) & 1) * 16384 + (j & 1) * 8192; };
    auto slotA = [&](auto ST, f32x16& Sc, f32x16& Sn, int j, const char* src, unsigned m0v) {
        LAS const char* va = vaddr(j);
        float ea = 0.f, eb = 0.f;
#pragma unroll
        for (int i = 0; i < 4; ++i) pk[i] = pn[i];
#pragma unroll
        for (int i = 0; i < 8; ++i) {
            Sn = mfma32(fr[i], qf[i], i ? Sn : negm);
            fr[i] = lds_b128(va + i * 1024);
            if (!(i & 1)) { ea = XEXP(Sc[8 + i]); eb = XEXP(Sc[9 + i]); PIN(ea); PIN(eb); if (ST.value) piece(src, m0v, i >> 1); }
            else { pk[4 + (i >> 1)] = cvtpk(ea, eb); t0 += ea; t1 += eb; PIN(t0); PIN(t1); }
            SBAR();
        }
        float ts = t0 + t1;
        if (__builtin_expect(__any(!(ts < 1.f)), 0)) {
            const float delta = fmaxf(swap32_max(max16(Sc)) + SM_BIAS, 0.f), alpha = __builtin_amdgcn_exp2f(-delta);
            l *= alpha;
#pragma unroll
            for (int d = 0; d < 4; ++d) o[d] *= alpha;
            negm -= delta; Sn -= delta; t0 = 0.f; t1 = 0.f;
#pragma unroll
            for (int i = 0; i < 8; ++i) { const float a = __builtin_amdgcn_exp2f(Sc[2 * i] - delta), b = __builtin_amdgcn_exp2f(Sc[2 * i + 1] - delta); pk[i] = cvtpk(a, b); t0 += a; t1 += b; }
            ts = t0 + t1;
        }
        l += ts;
        if (j + 1 == tbq) diag_mask(Sn, r, h);
    };
    auto slotB = [&](auto ST, f32x16& Sn, int j, const char* src, unsigned m0v) {
        LAS const char* ka = kaddr(j + 2);
        float ea = 0.f, eb = 0.f;
        t0 = 0.f; t1 = 0.f;
#pragma unroll
        for (int i = 0; i < 8; ++i) {
            o[i & 3] = mfma32(fr[i], pfrag(pk, 4 * (i >> 2)), o[i & 3]);
            fr[i] = lds_b128(ka + i * 1024);
            if (!(i & 1)) { if (ST.value) piece(src, m0v, i >> 1); ea = XEXP(Sn[i]); eb = XEXP(Sn[i + 1]); PIN(ea); PIN(eb); }
            else { pn[i >> 1] = cvtpk(ea, eb); t0 += ea; t1 += eb; PIN(pn[i >> 1]); PIN(t0); PIN(t1); }
            SBAR();
        }
    };
    using TT = std::integral_constant<bool, true>; using FF = std::integral_constant<bool, false>;
    WAIT_BAR(0);
    {
        S0 = f32x16{};
#pragma unroll
        for (int s = 0; s < 8; ++s) fr[s] = lds_b128(lbase + s * 1024);
#pragma unroll
        for (int s = 0; s < 8; ++s) S0 = mfma32(fr[s], qf[s], S0);
#pragma unroll
        for (int s = 0; s < 8; ++s) fr[s] = lds_b128(lbase + 8192 + s * 1024);
        if (tbq == 0) diag_mask(S0, r, h);
        const float nm = -(swap32_max(max16(S0)) + SM_BIAS);
#pragma unroll
        for (int i = 0; i < 16; ++i) negm[i] = nm;
        S0 += nm;
        t0 = 0.f; t1 = 0.f;
#pragma unroll
        for (int i = 0; i < 4; ++i) { const float a = __builtin_amdgcn_exp2f(S0[2 * i]), b = __builtin_amdgcn_exp2f(S0[2 * i + 1]); pn[i] = cvtpk(a, b); t0 += a; t1 += b; }
    }
    for (int j = 0;; j += 2) {
        const int t = j >> 1;
        WAIT_BAR(0);
#define BPRIO_MASK 3
        if ((t & BPRIO_MASK) != BPRIO_MASK || BPRIO_MASK == 0) __builtin_amdgcn_s_setprio(2); else __builtin_amdgcn_s_setprio(0);
        const int tk = t + 2 < NT ? t + 2 : NT - 1, tv = t + 1 < NT ? t + 1 : NT - 1;
        slotA(TT{}, S0, S1, j, kbase + (size_t)tk * 16384, lds0 + (t & 1) * 16384);  slotB(TT{}, S1, j, vbase + (size_t)tv * 16384, lds0 + 32768 + ((t + 1) & 1) * 16384);
        if (j == tbq) break;
        slotA(FF{}, S1, S0, j + 1, nullptr, 0u); slotB(FF{}, S0, j + 1, nullptr, 0u);
        if (j + 1 == tbq) break;
    }
    if (wid < 2) WAIT_BAR(0);
    asm volatile("s_waitcnt vmcnt(0)" ::: "memory");
    const float inv = 1.f / swap32_add(l);
    char* dst = ws + WS_CTX + (size_t)(tbq * 256 + hd * 8) * 1024 + lane * 16;
#pragma unroll
    for (int d = 0; d < 4; ++d) { st16(dst + (2 * d) * 1024, pack8(o[d], 0, inv)); st16(dst + (2 * d + 1) * 1024, pack8(o[d], 8, inv)); }
}

DI void projA(LAS char* lds, const P2& p, int hg, int tg, bf16x8 (&qf)[2][2], int wid, int lane) {
    constexpr int STG = 24576, NST = 16;
    const int r = lane & 31, h = lane >> 5;
    const char* xsrc = (const char*)p.xA + (size_t)tg * 64 * 2048;
    unsigned voX[2], voW[4];
#pragma unroll
    for (int i = 0; i < 2; ++i) { const int row = (wid * 2 + i) * 8 + (lane >> 3), ch = (lane & 7) ^ ((row >> 1) & 7); voX[i] = (unsigned)row * 2048u + ch * 16; }
#pragma unroll
    for (int i = 0; i < 4; ++i) { const int k = ((wid & 1) * 4 + i) * 8 + (lane >> 3), slot = lane & 7;
        voW[i] = (unsigned)k * 2048u + (128 * hg + 64 * (wid >> 1) + 32 * ((slot >> 2) ^ ((k >> 1) & 1)) + 8 * (slot & 3)) * 2; }
    auto stage1 = [&](int slot, int t, int i) {
        LAS char* base = lds + slot * STG;
        if (i < 2) glds16(xsrc + (size_t)t * 128, voX[i], base + (wid * 2 + i) * 1024);
        else glds16((const char*)p.wqA + (size_t)t * 64 * 2048, voW[i - 2], base + 8192 + (wid >> 1) * 8192 + ((wid & 1) * 4 + i - 2) * 1024);
    };
    int aoff[4];
#pragma unroll
    for (int s = 0; s < 4; ++s) aoff[s] = r * 128 + (((2 * s + h) ^ ((r >> 1) & 7)) << 4);
    const int blk = (lane >> 4) & 1, q = (lane & 15) >> 2, pq = lane & 3;
    const int boff = 8192 + (wid >> 1) * 8192 + (8 * h + q) * 128 + 64 * ((wid & 1) ^ (q >> 1)) + 32 * blk + 8 * pq;
    f32x16 acc[2]; acc[0] = f32x16{}; acc[1] = f32x16{};
    bf16x8 wf[4], a0[4], a1[4];
    auto rd = [&](int slot, int s) { LAS const char* sa = lds + slot * STG;
        wf[s] = cat8(lds_tr(sa + boff + s * 2048), lds_tr(sa + boff + s * 2048 + 512)); a0[s] = lds_b128(sa + aoff[s]); a1[s] = lds_b128(sa + aoff[s] + 4096); };
#pragma unroll
    for (int t = 0; t < 3; ++t)
#pragma unroll
        for (int i = 0; i < 6; ++i) stage1(t, t, i);
    WAIT_BAR(12);
    rd(0, 0); rd(0, 1);
    int slot = 0;
    for (int t = 0; t < NST; ++t) {
        int sn = slot + 1; if (sn == 3) sn = 0;
        rd(slot, 2); acc[0] = mfma32(wf[0], a0[0], acc[0]); acc[1] = mfma32(wf[0], a1[0], acc[1]); SBAR();
        rd(slot, 3); acc[0] = mfma32(wf[1], a0[1], acc[0]); acc[1] = mfma32(wf[1], a1[1], acc[1]); SBAR();
        if (t + 2 < NST) { WAIT_BAR(6); } else { WAIT_BAR(0); }
        rd(sn, 0); acc[0] = mfma32(wf[2], a0[2], acc[0]); acc[1] = mfma32(wf[2], a1[2], acc[1]); SBAR();
        if (t + 3 < NST) { stage1(slot, t + 3, 0); stage1(slot, t + 3, 1); stage1(slot, t + 3, 2); }
        rd(sn, 1); acc[0] = mfma32(wf[3], a0[3], acc[0]); acc[1] = mfma32(wf[3], a1[3], acc[1]); SBAR();
        if (t + 3 < NST) { stage1(slot, t + 3, 3); stage1(slot, t + 3, 4); stage1(slot, t + 3, 5); }
        slot = sn;
    }
#pragma unroll
    for (int u = 0; u < 2; ++u)
#pragma unroll
        for (int s = 0; s < 2; ++s) qf[u][s] = __builtin_bit_cast(bf16x8, pack8(acc[u], 8 * s, CA));
    WAIT_BAR(0);
}
DI void attnA(LAS char* lds, const P2& p, int hg, int tg, int wid, int lane) {
    char* ws = p.ws;
    const int hd = 4 * hg + wid, tb0 = 2 * tg;
    bf16x8 qf[2][2];
    __builtin_amdgcn_s_setprio(3);
    projA(lds, p, hg, tg, qf, wid, lane);
    STAMP(ws, 1, 1);
    __builtin_amdgcn_s_setprio(1);
    STAMP(ws, 1, 2);
    const char* kvsrc = ws + ((wid & 2) ? WS_VA : WS_KA) + (wid & 1) * 4096;
    auto stage = [&](int slot, int t) { glds16x4(kvsrc + (size_t)t * 8192, lane * 16, lds + slot * 16384 + wid * 4096); };
    stage(0, 0); stage(1, 1);
    f32x16 o[2], negm[2], Sa[2], Sb[2]; float l[2] = {0.f, 0.f};
    o[0] = f32x16{}; o[1] = f32x16{};
    bf16x8 kf[2], vf[2]; unsigned pp[2][8];
    LAS const char* lbase = lds + lane * 16;
    auto step = [&](f32x16 (&Sc)[2], f32x16 (&Sn)[2], LAS const char* ka, LAS const char* va) {
        unsigned pn[2][8]; float t[2][2] = {{0.f, 0.f}, {0.f, 0.f}}, e[2][4];
#pragma unroll
        for (int i = 0; i < 9; ++i) {
            if (i < 8) {
                const int u = i & 1, s = (i >> 1) & 1;
                if (i < 4) Sn[u] = mfma32(kf[s], qf[u][s], s ? Sn[u] : negm[u]);
                else o[u] = mfma32(vf[s], pfrag(pp[u], 4 * s), o[u]);
                if (i == 3) { kf[0] = lds_b128(ka); kf[1] = lds_b128(ka + 1024); }
                if (i == 7) { vf[0] = lds_b128(va); vf[1] = lds_b128(va + 1024); }
#pragma unroll
                for (int k = 0; k < 4; ++k) { e[i & 1][k] = __builtin_amdgcn_exp2f(Sc[i >> 2][(i & 3) * 4 + k]); PIN(e[i & 1][k]); }
            }
            if (i > 0) {
                const int g = i - 1, uu = g >> 2, w = (g & 3) * 2; const float* x = e[g & 1];
                pn[uu][w] = cvtpk(x[0], x[1]); pn[uu][w + 1] = cvtpk(x[2], x[3]);
                t[uu][0] += x[0]; t[uu][1] += x[1]; t[uu][0] += x[2]; t[uu][1] += x[3]; PIN(t[uu][0]); PIN(t[uu][1]); PIN(pn[uu][w]); PIN(pn[uu][w + 1]);
            }
            if (i < 8) SBAR();
        }
        float ts[2] = {t[0][0] + t[0][1], t[1][0] + t[1][1]};
        if (__builtin_expect(__any(!(ts[0] + ts[1] < 1.f)), 0)) {
#pragma unroll
            for (int u = 0; u < 2; ++u) {
                const float dl = fmaxf(swap32_max(max16(Sc[u])) + SM_BIAS, 0.f), al = __builtin_amdgcn_exp2f(-dl);
                l[u] *= al; o[u] *= al; negm[u] -= dl; Sn[u] -= dl; float a0 = 0.f, a1 = 0.f;
#pragma unroll
                for (int i = 0; i < 8; ++i) { const float a = __builtin_amdgcn_exp2f(Sc[u][2 * i] - dl), b = __builtin_amdgcn_exp2f(Sc[u][2 * i + 1] - dl); pn[u][i] = cvtpk(a, b); a0 += a; a1 += b; }
                ts[u] = a0 + a1;
            }
        }
#pragma unroll
        for (int u = 0; u < 2; ++u) { l[u] += ts[u];
#pragma unroll
            for (int i = 0; i < 8; ++i) pp[u][i] = pn[u][i]; }
    };
    WAIT_BAR(4);
    {
        kf[0] = lds_b128(lbase); kf[1] = lds_b128(lbase + 1024);
#pragma unroll
        for (int u = 0; u < 2; ++u) {
            Sa[u] = mfma32(kf[0], qf[u][0], f32x16{}); Sa[u] = mfma32(kf[1], qf[u][1], Sa[u]);
            const float nm = -(swap32_max(max16(Sa[u])) + SM_BIAS);
#pragma unroll
            for (int i = 0; i < 16; ++i) negm[u][i] = nm;
            Sa[u] += nm;
#pragma unroll
            for (int i = 0; i < 8; ++i) pp[u][i] = 0u;
        }
        kf[0] = lds_b128(lbase + 2048); kf[1] = lds_b128(lbase + 2048 + 1024);
        vf[0] = kf[0]; vf[1] = kf[1];
    }
    int slot = 0;
    for (int t = 0; t < 16; ++t) {
        WAIT_BAR(0);
#define APRIO_T 99
        if (t == APRIO_T) __builtin_amdgcn_s_setprio(3);
        int sn = slot + 1; if (sn == 3) sn = 0; int sp = sn + 1; if (sp == 3) sp = 0;
        if (t + 2 < 16) stage(sp, t + 2);
        if (t == 2) { const int gw = (hg * 32 + tg) * 4 + wid; l2pf((const char*)p.woB + (size_t)gw * 8192, lane * 128, lds + 49152 + wid * 256);
                      l2pf((const char*)p.woA + (size_t)gw * 2048, (lane & 15) * 128, lds + 49152 + wid * 256); }
        LAS const char* cur = lbase + slot * 16384; LAS const char* nxt = lbase + sn * 16384;
        step(Sa, Sb, cur + 2 * 2048, cur + 8192);
        step(Sb, Sa, cur + 3 * 2048, cur + 8192 + 2048);
        step(Sa, Sb, nxt, cur + 8192 + 2 * 2048);
        step(Sb, Sa, nxt + 2048, cur + 8192 + 3 * 2048);
        slot = sn;
    }
#pragma unroll
    for (int s = 0; s < 2; ++s)
#pragma unroll
        for (int u = 0; u < 2; ++u) o[u] = mfma32(vf[s], pfrag(pp[u], 4 * s), o[u]);
#pragma unroll
    for (int u = 0; u < 2; ++u) {
        const float inv = 1.f / swap32_add(l[u]);
        char* dst = ws + WS_OA + (size_t)((tb0 + u) * 64 + hd * 2) * 1024 + lane * 16;
        st16(dst, pack8(o[u], 0, inv)); st16(dst + 1024, pack8(o[u], 8, inv));
    }
}

__global__ __launch_bounds__(256, 2) void k_attn(P2 p) {
    extern __shared__ __attribute__((aligned(16))) char smem[];
    LAS char* lds = (LAS char*)smem;
    const int tid = threadIdx.x, wid = __builtin_amdgcn_readfirstlane(tid >> 6), lane = tid & 63;
    const int bid = blockIdx.x;
    const int xcd = bid & 7, idx = bid >> 3,
        kind = (idx ^ (idx >> 5)) & 1,
        it = (idx >> 1) & 15 | (idx >> 5) << 4;
    STAMP(p.ws, 1, 0);
    if (kind == 0) { attnA(lds, p, xcd, it, wid, lane); }
    else {
        const int hd = xcd + 8 * (it >> 3), x = it & 7;
        attnB(lds, p.ws, hd, 15 - x, wid, lane);
        WAIT_BAR(0);
        STAMP(p.ws, 1, 1);
        attnB(lds, p.ws, hd, x, wid, lane); STAMP(p.ws, 1, 2); }
    STAMP(p.ws, 1, 3);
}

struct P3 { const u16 *woA, *woB; char* ws; u16* out; };

constexpr int NT1 = 24;
DI void out_store(u16* out, int tb0, int col0, const f32x16 (&acc)[2][2], int wid, int lane) {
    const int wm = wid >> 1, wn = wid & 1, r = lane & 31, h = lane >> 5;
#pragma unroll
    for (int m = 0; m < 2; ++m) {
        char* dst = (char*)out + (size_t)((tb0 + 2 * wm + m) * 32 + r) * 4096 + (col0 + 64 * wn + 8 * h) * 2;
#pragma unroll
        for (int c = 0; c < 2; ++c) { *(u32x4*)(dst + c * 64) = pack8(acc[m][c], 0, 1.f); *(u32x4*)(dst + c * 64 + 32) = pack8(acc[m][c], 8, 1.f); }
    }
}

__global__ __launch_bounds__(512, 2) void k_out(P3 p) {
    extern __shared__ __attribute__((aligned(16))) char smem[];
    LAS char* lds = (LAS char*)smem;
    constexpr int STG = 32768, nt = 40;
    const int tid = threadIdx.x, wid = __builtin_amdgcn_readfirstlane(tid >> 6), lane = tid & 63;
    const int bid = blockIdx.x, xg = bid & 7, role = (xg >> 2) ^ 1, ti = bid >> 3;
    const int pm = 8 * ((xg >> 1) & 1) + (ti >> 2), pn = 4 * (xg & 1) + (ti & 3), tb0 = pm * 4, tau = pm * 8 + pn;
    STAMP(p.ws, 2, 0);
    if (wid >= 4) {
        const int lw = wid - 4;
        const char* a0 = p.ws + WS_CTX + (size_t)((tb0 + lw) * 256 + (role ? 4 * (64 - NT1) : 0)) * 1024;
        const char* w0 = (const char*)p.woB + pn * 256 + (size_t)(role ? 64 - NT1 : 0) * 64 * 2048;
        const char* a1 = p.ws + WS_OA + (size_t)((tb0 + lw) * 64) * 1024 - (size_t)NT1 * 4096;
        const char* w1 = (const char*)p.woA + pn * 256 - (size_t)NT1 * 64 * 2048;
        const int tsw = role ? NT1 : nt;
        unsigned voW[4];
#pragma unroll
        for (int i = 0; i < 4; ++i) { const int pc = 4 * lw + i, g = pc >> 3, k = (pc & 7) * 8 + (lane >> 3), slot = lane & 7;
            const int col = 64 * g + 32 * ((slot >> 2) ^ ((k >> 1) & 1)) + 8 * (slot & 3); voW[i] = (unsigned)k * 2048u + col * 2; }
        auto stage = [&](int t) {
            LAS char* base = lds + (t & 3) * STG;
            const char* as = (t < tsw ? a0 : a1) + (size_t)t * 4096; const char* wsd = (t < tsw ? w0 : w1) + (size_t)t * (64 * 2048);
            glds16x4(as, lane * 16, base + lw * 4096);
#pragma unroll
            for (int i = 0; i < 4; ++i) glds16(wsd, voW[i], base + 16384 + (4 * lw + i) * 1024);
        };
#pragma unroll
        for (int t = 0; t < 4; ++t) stage(t);
        WAIT_BAR(24);
        for (int t = 0; t < nt; ++t) {
            if (t + 3 < nt) { WAIT_BAR(16); } else if (t + 2 < nt) { WAIT_BAR(8); } else { WAIT_BAR(0); }
            if (t + 4 < nt) stage(t + 4);
        }
    } else {
        const int wm = wid >> 1, wn = wid & 1, h = lane >> 5;
        const int blk = (lane >> 4) & 1, q = (lane & 15) >> 2, pq = lane & 3, pp = ((pq & 1) << 1) | (pq >> 1);
        GAS unsigned* flag = (GAS unsigned*)(p.ws + WS_FLAG) + tau * 16;
        const __amdgpu_buffer_rsrc_t rs = __builtin_amdgcn_make_buffer_rsrc(p.ws + WS_PART + (size_t)tau * 65536, 0, 65536, 0x00020000);
        int boff[2];
#pragma unroll
        for (int c = 0; c < 2; ++c) boff[c] = 16384 + wn * 8192 + (4 * h + q) * 128 + 64 * (c ^ (q >> 1)) + 32 * blk + 8 * pp;
        const int aoff = (2 * wm * 4) * 1024 + lane * 16;
        f32x16 acc[2][2];
#pragma unroll
        for (int m = 0; m < 2; ++m) { acc[m][0] = f32x16{}; acc[m][1] = f32x16{}; }
        bf16x8 af[4][2], wf[4][2];
        auto rd = [&](int slot, int s, int i) {
            LAS const char* sa = lds + slot * STG;
            if (i & 1) af[s][i >> 1] = lds_b128(sa + aoff + ((i >> 1) * 4 + s) * 1024);
            else wf[s][i >> 1] = cat8(lds_tr(sa + boff[i >> 1] + s * 2048), lds_tr(sa + boff[i >> 1] + s * 2048 + 1024));
        };
        auto kstep = [&](int s, int nslot) {
#pragma unroll
            for (int i = 0; i < 4; ++i) {
                const int m = i >> 1, c = i & 1;
                acc[m][c] = mfma32(wf[s][c], af[s][m], acc[m][c]);
                rd(nslot, (s + 2) & 3, i);
                SBAR();
            }
        };
        auto tiles = [&](int t0, int t1) {
#pragma unroll 1
            for (int t = t0; t < t1; ++t) {
                kstep(0, t & 3); kstep(1, t & 3);
                asm volatile("s_waitcnt lgkmcnt(0)\n\ts_barrier" ::: "memory");
                kstep(2, (t + 1) & 3); kstep(3, (t + 1) & 3);
            }
        };
        WAIT_BAR(0);
#pragma unroll
        for (int i = 0; i < 4; ++i) { rd(0, 0, i); rd(0, 1, i); }
        if (role == 1) {
            tiles(0, NT1);
#pragma unroll
            for (int m = 0; m < 2; ++m)
#pragma unroll
                for (int c = 0; c < 2; ++c) {
#pragma unroll
                    for (int i = 0; i < 4; ++i) { u32x4 v;
#pragma unroll
                        for (int j = 0; j < 4; ++j) v[j] = __float_as_uint(acc[m][c][4 * i + j]);
                        __builtin_amdgcn_raw_buffer_store_b128(v, rs, wid * 16384 + ((m * 2 + c) * 4 + i) * 1024 + lane * 16, 0, 16); }
                    acc[m][c] = f32x16{}; }
            tiles(NT1, NT1 + 4);
            asm volatile("s_waitcnt vmcnt(0)" ::: "memory");
            tiles(NT1 + 4, NT1 + 5);
            if (tid == 0) __hip_atomic_store(flag, 1u, __ATOMIC_RELAXED, __HIP_MEMORY_SCOPE_AGENT);
            STAMP(p.ws, 2, 1);
            tiles(NT1 + 5, nt);
            STAMP(p.ws, 2, 2);
            out_store(p.out, tb0, pn * 128, acc, wid, lane);
        } else {
            tiles(0, nt - 5);
            while ((unsigned)__builtin_amdgcn_readfirstlane(__hip_atomic_load(flag, __ATOMIC_RELAXED, __HIP_MEMORY_SCOPE_AGENT)) != 1u) __builtin_amdgcn_s_sleep(2);
            __builtin_amdgcn_fence(__ATOMIC_ACQUIRE, "wavefront");
            u32x4 pv[16];
#pragma unroll
            for (int i = 0; i < 16; ++i) pv[i] = __builtin_amdgcn_raw_buffer_load_b128(rs, wid * 16384 + i * 1024 + lane * 16, 0, 16);
            STAMP(p.ws, 2, 1);
            tiles(nt - 5, nt);
#pragma unroll
            for (int i = 0; i < 16; ++i)
#pragma unroll
                for (int j = 0; j < 4; ++j) acc[i >> 3][(i >> 2) & 1][4 * (i & 3) + j] += __uint_as_float(pv[i][j]);
            STAMP(p.ws, 2, 2);
            out_store(p.out, tb0, 1024 + pn * 128, acc, wid, lane);
        }
        STAMP(p.ws, 2, 3);
    }
}

#define BENCH_MARK(s)
extern "C" void kernel_launch(void* const* d_in, const int* in_sizes, int n_in, void* d_out, int out_size, void* d_ws, size_t ws_size,
                              hipStream_t stream) {
    (void)in_sizes; (void)n_in; (void)out_size; (void)ws_size;
    (void)hipFuncSetAttribute((const void*)k_proj, hipFuncAttributeMaxDynamicSharedMemorySize, 133120);
    (void)hipFuncSetAttribute((const void*)k_attn, hipFuncAttributeMaxDynamicSharedMemorySize, 73728);
    (void)hipFuncSetAttribute((const void*)k_out, hipFuncAttributeMaxDynamicSharedMemorySize, 131072);
    P1 p1; p1.wqA = (const u16*)d_in[1]; p1.xA = (const u16*)d_in[0]; p1.wkA = (const u16*)d_in[2]; p1.wvA = (const u16*)d_in[3];
    p1.xB = (const u16*)d_in[5]; p1.cKV = (const u16*)d_in[6]; p1.wqB = (const u16*)d_in[7]; p1.wukB = (const u16*)d_in[8]; p1.wuvB = (const u16*)d_in[9];
    p1.ws = (char*)d_ws;
    BENCH_MARK(stream);
    k_proj<<<256, 512, 133120, stream>>>(p1);
    BENCH_MARK(stream);
    P2 p2; p2.woA = (const u16*)d_in[4]; p2.woB = (const u16*)d_in[10]; p2.xA = (const u16*)d_in[0]; p2.wqA = (const u16*)d_in[1]; p2.ws = (char*)d_ws;
    k_attn<<<512, 256, 73728, stream>>>(p2);
    BENCH_MARK(stream);
    P3 p3; p3.woA = (const u16*)d_in[4]; p3.woB = (const u16*)d_in[10]; p3.ws = (char*)d_ws; p3.out = (u16*)d_out;
    k_out<<<256, 512, 131072, stream>>>(p3);
    BENCH_MARK(stream);
}
```

```cpp
#include <hip/hip_runtime.h>
#include <hip/hip_bf16.h>
#include <type_traits>

typedef unsigned short u16;
typedef short bf16x8 __attribute__((ext_vector_type(8)));
typedef short bf16x4 __attribute__((ext_vector_type(4)));
typedef float f32x16 __attribute__((ext_vector_type(16)));
typedef float f32x4 __attribute__((ext_vector_type(4)));
typedef float f32x2 __attribute__((ext_vector_type(2)));
typedef __bf16 bf16x2_t __attribute__((ext_vector_type(2)));
typedef unsigned u32x4 __attribute__((ext_vector_type(4)));
#define LAS __attribute__((address_space(3)))
#define GAS __attribute__((address_space(1)))
#define DI __device__ __forceinline__

DI f32x16 mfma32(bf16x8 a, bf16x8 b, f32x16 c) { return __builtin_amdgcn_mfma_f32_32x32x16_bf16(a, b, c, 0, 0, 0); }
DI unsigned cvtpk(float lo, float hi) { f32x2 v = {lo, hi}; bf16x2_t b = __builtin_convertvector(v, bf16x2_t); return __builtin_bit_cast(unsigned, b); }
DI void glds16(const void* sbase, unsigned voff, LAS void* l) {
    unsigned sv; asm volatile("s_mov_b32 %0, m0\n\ts_mov_b32 m0, %3\n\ts_nop 0\n\tglobal_load_lds_dwordx4 %1, %2\n\ts_mov_b32 m0, %0"
                              : "=&s"(sv) : "v"(voff), "s"(sbase), "s"((unsigned)(size_t)l) : "memory"); }
template <int OFF> DI void glds16o(const void* sbase, unsigned voff, unsigned m0v) {
    asm volatile("s_mov_b32 m0, %2\n\ts_nop 0\n\tglobal_load_lds_dwordx4 %0, %1 offset:%3" :: "v"(voff), "s"(sbase), "s"(m0v), "i"(OFF) : "memory"); }
DI void l2pf(const void* sbase, unsigned voff, LAS void* scratch) {
    unsigned sv; asm volatile("s_mov_b32 %0, m0\n\ts_mov_b32 m0, %3\n\ts_nop 0\n\tglobal_load_lds_dword %1, %2\n\ts_mov_b32 m0, %0"
                              : "=&s"(sv) : "v"(voff), "s"(sbase), "s"((unsigned)(size_t)scratch) : "memory"); }
DI void glds16v(const void* vaddr, LAS void* l) {
    unsigned sv; asm volatile("s_mov_b32 %0, m0\n\ts_mov_b32 m0, %2\n\ts_nop 0\n\tglobal_load_lds_dwordx4 %1, off\n\ts_mov_b32 m0, %0"
                              : "=&s"(sv) : "v"(vaddr), "s"((unsigned)(size_t)l) : "memory"); }
DI void glds16x4(const void* sbase, unsigned voff, LAS void* l) {
    unsigned sv; asm volatile("s_mov_b32 %0, m0\n\ts_mov_b32 m0, %3\n\ts_nop 0\n\tglobal_load_lds_dwordx4 %1, %2\n\tglobal_load_lds_dwordx4 %1, %2 offset:1024\n\t"
                              "global_load_lds_dwordx4 %1, %2 offset:2048\n\tglobal_load_lds_dwordx4 %1, %2 offset:3072\n\ts_mov_b32 m0, %0"
                              : "=&s"(sv) : "v"(voff), "s"(sbase), "s"((unsigned)(size_t)l) : "memory"); }
DI void glds16x2(const void* sbase, unsigned voff, LAS void* l) {
    unsigned sv; asm volatile("s_mov_b32 %0, m0\n\ts_mov_b32 m0, %3\n\ts_nop 0\n\tglobal_load_lds_dwordx4 %1, %2\n\tglobal_load_lds_dwordx4 %1, %2 offset:1024\n\ts_mov_b32 m0, %0"
                              : "=&s"(sv) : "v"(voff), "s"(sbase), "s"((unsigned)(size_t)l) : "memory"); }
DI bf16x8 lds_b128(LAS const char* p) { return *(LAS const bf16x8*)p; }
DI bf16x4 lds_tr(LAS const char* p) { return __builtin_amdgcn_ds_read_tr16_b64_v4i16((LAS bf16x4*)p); }
DI bf16x8 cat8(bf16x4 a, bf16x4 b) { return (bf16x8){a[0], a[1], a[2], a[3], b[0], b[1], b[2], b[3]}; }
DI float swap32_max(float x) { auto rr = __builtin_amdgcn_permlane32_swap(__float_as_uint(x), __float_as_uint(x), false, false); return fmaxf(__uint_as_float(rr[0]), __uint_as_float(rr[1])); }
DI float swap32_add(float x) { auto rr = __builtin_amdgcn_permlane32_swap(__float_as_uint(x), __float_as_uint(x), false, false); return __uint_as_float(rr[0]) + __uint_as_float(rr[1]); }
#define WAIT_BAR(N) asm volatile("s_waitcnt vmcnt(" #N ") lgkmcnt(0)\n\ts_barrier" ::: "memory")
#define SBAR() __builtin_amdgcn_sched_barrier(0)
#define PIN(x) asm volatile("" : "+v"(x))
#define WAIT_BAR_I(N) asm volatile("s_waitcnt vmcnt(%0) lgkmcnt(0)\n\ts_barrier" :: "i"(N) : "memory")

DI void st16(void* p, u32x4 v) { *(u32x4*)p = v; }
DI u32x4 pack8(const f32x16& a, int b, float sc) {
    u32x4 w; w.x = cvtpk(a[b + 0] * sc, a[b + 1] * sc); w.y = cvtpk(a[b + 2] * sc, a[b + 3] * sc);
    w.z = cvtpk(a[b + 4] * sc, a[b + 5] * sc); w.w = cvtpk(a[b + 6] * sc, a[b + 7] * sc); return w;
}

constexpr int SEQ = 2048, NTB = SEQ / 32;
constexpr float LOG2E = 1.4426950408889634f;
constexpr float CA = 0.17677669529663687f * LOG2E;
constexpr float CB = 0.08838834764831845f * LOG2E;
#define SMB 64.f
constexpr float SM_BIAS = SMB;

constexpr size_t WS_QB = 0, WS_KB = WS_QB + (16u << 20), WS_VB = WS_KB + (16u << 20), WS_QA = WS_VB + (16u << 20),
                 WS_KA = WS_QA + (4u << 20), WS_VA = WS_KA + (128u << 10), WS_CTX = WS_VA + (128u << 10), WS_OA = WS_CTX + (16u << 20),
                 WS_PART = WS_OA + (4u << 20), WS_FLAG = WS_PART + (8u << 20), WS_END = WS_FLAG + 129 * 64;

#define STAMP(ws, kn, i)
struct P1 { const u16 *xB, *cKV, *wqB, *wukB, *wuvB, *xA, *wkA, *wvA, *wqA; char* ws; };

template <bool KV, class Mid>
DI void big_stream(LAS char* lds, const char* Ab, unsigned lda_b, const char* W0, const char* W1, f32x16 (&acc)[4][2], Mid mid, const char* warm, const char* warm2, unsigned w2off, const char* self, int wid, int lane) {
    constexpr unsigned ldw_b = 8192;
    const int wr = wid >> 2, wc = wid & 3, r = lane & 31, h = lane >> 5;
    unsigned voA[4], voW[2];
#pragma unroll
    for (int i = 0; i < 4; ++i) { const int row = (wid * 4 + i) * 8 + (lane >> 3), ch = (lane & 7) ^ ((row >> 1) & 7); voA[i] = (unsigned)row * lda_b + ch * 16; }
#pragma unroll
    for (int i = 0; i < 2; ++i) {
        const int k = ((wid & 1) * 2 + i) * 8 + (lane >> 3), slot = lane & 7;
        const int col = 64 * (wid >> 1) + 32 * ((slot >> 2) ^ ((k >> 1) & 1)) + 8 * (slot & 3);
        voW[i] = (unsigned)k * ldw_b + col * 2;
    }
    int aoff[4], boff[2];
#pragma unroll
    for (int s = 0; s < 4; ++s) aoff[s] = (128 * wr + r) * 128 + (((2 * s + h) ^ ((r >> 1) & 7)) << 4);
    { const int blk = (lane >> 4) & 1, q = (lane & 15) >> 2, p = lane & 3;
#pragma unroll
      for (int n = 0; n < 2; ++n) boff[n] = 65536 + wc * 4096 + (8 * h + q) * 128 + 64 * (n ^ (q >> 1)) + 32 * blk + 8 * p; }
#pragma unroll
    for (int m = 0; m < 4; ++m) { acc[m][0] = f32x16{}; acc[m][1] = f32x16{}; }
    auto stA = [&](int pr, int i) { glds16(Ab + (size_t)(KV ? (pr & 7) : pr) * 128, voA[i], lds + (pr & 1) * 32768 + (wid * 4 + i) * 1024); };
    auto stW = [&](int t, int i) { glds16(((KV && t >= 16) ? W1 : W0) + (size_t)(KV ? (t & 15) : t) * 32 * ldw_b, voW[i], lds + 65536 + (t & 3) * 16384 + (wid >> 1) * 4096 + ((wid & 1) * 2 + i) * 1024); };
    bf16x8 af[2][4], wf[2][2];
    auto rd = [&](int rs, int t, int par, int s, int i) {
        if (i < 2) { LAS const char* sw = lds + (t & 3) * 16384; wf[rs][i] = cat8(lds_tr(sw + boff[i] + s * 2048), lds_tr(sw + boff[i] + s * 2048 + 512)); }
        else af[rs][i - 2] = lds_b128(lds + ((t >> 1) & 1) * 32768 + aoff[2 * par + s] + (i - 2) * 4096);
    };
    auto ks0 = [&](auto OT, int t, int par) {
#pragma unroll
        for (int i = 0; i < 8; ++i) { const int m = i >> 1, n = i & 1;
            acc[m][n] = OT.value ? mfma32(wf[0][n], af[0][m], acc[m][n]) : mfma32(af[0][m], wf[0][n], acc[m][n]);
            if (i < 6) rd(1, t, par, 1, i);
            SBAR(); }
    };
    auto ks1 = [&](auto OT, int t, int par, bool ld) {
#pragma unroll
        for (int i = 0; i < 8; ++i) { const int m = i >> 1, n = i & 1;
            acc[m][n] = OT.value ? mfma32(wf[1][n], af[1][m], acc[m][n]) : mfma32(af[1][m], wf[1][n], acc[m][n]);
            if (i < 6) rd(0, t + 1, par ^ 1, 0, i);
            if (ld) { if (par) { if (i >= 1 && i <= 4) stA((t + 3) >> 1, i - 1); if (i >= 5 && i <= 6) stW(t + 4, i - 5); } else if (i == 2 || i == 5) stW(t + 4, i == 5); }
            SBAR(); }
    };
    using TT = std::integral_constant<bool, true>; using FF = std::integral_constant<bool, false>;
#pragma unroll
    for (int pr = 0; pr < 2; ++pr) {
#pragma unroll
        for (int i = 0; i < 4; ++i) stA(pr, i);
        stW(2 * pr, 0); stW(2 * pr, 1); stW(2 * pr + 1, 0); stW(2 * pr + 1, 1); }
    WAIT_BAR(10);
#pragma unroll
    for (int i = 0; i < 6; ++i) rd(0, 0, 0, 0, i);
#pragma unroll 1
    for (int t = 0; t < 12; t += 2) {
        ks0(TT{}, t, 0);     WAIT_BAR(8); ks1(TT{}, t, 0, true);
        ks0(TT{}, t + 1, 1); WAIT_BAR(4); ks1(TT{}, t + 1, 1, true);
    }
    ks0(TT{}, 12, 0); WAIT_BAR(8);
    l2pf(warm, (lane < 24 ? lane : 0) * 128, lds + 131072 + wid * 256); l2pf(warm2, w2off, lds + 131072 + wid * 256);
    ks1(TT{}, 12, 0, true);
    ks0(TT{}, 13, 1); WAIT_BAR(6);  ks1(TT{}, 13, 1, true);
    ks0(TT{}, 14, 0); WAIT_BAR(10); ks1(TT{}, 14, 0, true);
    ks0(TT{}, 15, 1); WAIT_BAR(4);  ks1(TT{}, 15, 1, true);
    if constexpr (!KV) {
#pragma unroll 1
        for (int t = 16; t < 28; t += 2) {
            ks0(TT{}, t, 0);     WAIT_BAR(8); ks1(TT{}, t, 0, true);
            ks0(TT{}, t + 1, 1); WAIT_BAR(4); ks1(TT{}, t + 1, 1, true);
        }
        ks0(TT{}, 28, 0); WAIT_BAR(8);  ks1(TT{}, 28, 0, false);
        ks0(TT{}, 29, 1); WAIT_BAR(2);  mid(0); mid(1); ks1(TT{}, 29, 1, false);
        ks0(TT{}, 30, 0); WAIT_BAR(16); ks1(TT{}, 30, 0, false);
        ks0(TT{}, 31, 1); ks1(TT{}, 30, 0, false);
    } else {
        mid(0);
#pragma unroll
        for (int m = 0; m < 4; ++m) { acc[m][0] = f32x16{}; acc[m][1] = f32x16{}; }
        ks0(FF{}, 16, 0); WAIT_BAR(24); ks1(FF{}, 16, 0, true);
        ks0(FF{}, 17, 1); WAIT_BAR(20); ks1(FF{}, 17, 1, true);
        ks0(FF{}, 18, 0); WAIT_BAR(24); ks1(FF{}, 18, 0, true);
        ks0(FF{}, 19, 1); WAIT_BAR(4);  ks1(FF{}, 19, 1, true);
#pragma unroll 1
        for (int t = 20; t < 28; t += 2) {
            ks0(FF{}, t, 0);     WAIT_BAR(8); ks1(FF{}, t, 0, true);
            ks0(FF{}, t + 1, 1); WAIT_BAR(4); ks1(FF{}, t + 1, 1, true);
        }
        ks0(FF{}, 28, 0); WAIT_BAR(8); ks1(FF{}, 28, 0, false);
        ks0(FF{}, 29, 1); WAIT_BAR(2); ks1(FF{}, 29, 1, false);
        ks0(FF{}, 30, 0); WAIT_BAR(0); ks1(FF{}, 30, 0, false);
        ks0(FF{}, 31, 1); ks1(FF{}, 30, 0, false);
    }
}
constexpr int KVP1 = 65536;

struct KvUnit {
    bf16x8 xf[8];
    DI void issue(int part, LAS char* lds, const P1& p, int tb, int isv, int wid, int lane) {
        const char* wsrc = (const char*)(isv ? p.wvA : p.wkA) + wid * 8192;
        glds16x4(wsrc + part * 4096, lane * 16, lds + part * KVP1 + wid * 4096);
        if (part) {
#pragma unroll
            for (int s = 0; s < 8; ++s) xf[s] = *(const bf16x8*)((const char*)p.xA + (size_t)(32 * tb + (lane & 31)) * 2048 + (128 * wid + 16 * s + 8 * (lane >> 5)) * 2);
        }
    }
    DI void finish(LAS char* lds, const P1& p, int tb, int isv, int wid, int lane) {
        const int h = lane >> 5, blk = (lane >> 4) & 1, q = (lane & 15) >> 2, pq = lane & 3;
        asm volatile("s_waitcnt vmcnt(16)" ::: "memory");
        f32x16 ak = f32x16{};
#pragma unroll
        for (int s = 0; s < 8; ++s) {
            LAS const char* b0 = lds + (s >> 2) * KVP1 + wid * 4096 + (16 * (s & 3) + 8 * h + q) * 64 + 32 * blk + 8 * pq;
            const bf16x8 wf = cat8(lds_tr(b0), lds_tr(b0 + 256));
            if (isv) ak = mfma32(xf[s], wf, ak); else ak = mfma32(wf, xf[s], ak);
        }
        asm volatile("s_waitcnt lgkmcnt(0)" ::: "memory");
#pragma unroll
        for (int i = 0; i < 4; ++i) *(LAS f32x4*)(lds + wid * 4096 + i * 1024 + lane * 16) = (f32x4){ak[4 * i], ak[4 * i + 1], ak[4 * i + 2], ak[4 * i + 3]};
        asm volatile("s_waitcnt lgkmcnt(0)\n\ts_barrier" ::: "memory");
        if (wid < 2) {
            f32x4 s0 = {0.f, 0.f, 0.f, 0.f}, s1 = {0.f, 0.f, 0.f, 0.f};
#pragma unroll
            for (int w = 0; w < 8; ++w) { s0 += *(LAS const f32x4*)(lds + w * 4096 + (2 * wid) * 1024 + lane * 16); s1 += *(LAS const f32x4*)(lds + w * 4096 + (2 * wid + 1) * 1024 + lane * 16); }
            u32x4 o; o.x = cvtpk(s0[0], s0[1]); o.y = cvtpk(s0[2], s0[3]); o.z = cvtpk(s1[0], s1[1]); o.w = cvtpk(s1[2], s1[3]);
            *(u32x4*)(p.ws + (isv ? WS_VA : WS_KA) + (size_t)(tb * 2 + wid) * 1024 + lane * 16) = o;
        }
    }
};

__global__ __launch_bounds__(512, 2) void k_proj(P1 p) {
    extern __shared__ __attribute__((aligned(16))) char smem[];
    LAS char* lds = (LAS char*)smem;
    const int tid = threadIdx.x, wid = __builtin_amdgcn_readfirstlane(tid >> 6), lane = tid & 63;
    const int bid = blockIdx.x, xcd = bid & 7, idx = bid >> 3;
    STAMP(p.ws, 0, 0);
    if (bid == 0 && tid < 128) ((unsigned*)(p.ws + WS_FLAG))[tid * 16] = 0u;
    const int wr = wid >> 2, wc = wid & 3;
    const int pm = idx & 7, pn = 2 * xcd + ((idx >> 3) & 1), kind = idx >> 4;
    const int gw = bid * 8 + wid;
    const char* warm = gw < 1365 ? (const char*)p.xA + (size_t)gw * 3072 : (const char*)p.wqA + (size_t)(gw - 1365 < 681 ? gw - 1365 : 681) * 3072;
    const char* self = gw < 1024 ? (const char*)p.wqB + (size_t)gw * 8192 : gw < 1536 ? (const char*)p.wukB + (size_t)(gw - 1024) * 8192 : (const char*)p.wuvB + (size_t)(gw - 1536) * 8192;
    f32x16 acc[4][2];
    auto store_f = [&](char* base, float sc) {
#pragma unroll
        for (int m = 0; m < 4; ++m)
#pragma unroll
            for (int n = 0; n < 2; ++n) { const int tb = pm * 8 + wr * 4 + m, fb = pn * 8 + wc * 2 + n, hd = fb >> 2, db = fb & 3;
                char* dst = base + (size_t)((hd * NTB + tb) * 8 + 2 * db) * 1024 + lane * 16;
                st16(dst, pack8(acc[m][n], 0, sc)); st16(dst + 1024, pack8(acc[m][n], 8, sc)); }
    };
    if (kind == 0) {
        KvUnit kv; const int tbk = xcd * 8 + pm, isv = (idx >> 3) & 1;
        big_stream<false>(lds, (const char*)p.xB + (size_t)pm * 256 * 2048, 2048, (const char*)p.wqB + pn * 512, nullptr, acc,
                          [&](int part) { kv.issue(part, lds, p, tbk, isv, wid, lane); }, warm, (const char*)(isv ? p.wvA : p.wkA) + wid * 8192, lane * 128, self, wid, lane);
        SBAR();
        store_f(p.ws + WS_QB, CB);
        SBAR();
        kv.finish(lds, p, tbk, isv, wid, lane);
    } else {
        unsigned long long pc; int oa, oo;
        asm volatile("s_getpc_b64 %0\n.Lpc_anchor_%=:\n\ts_mov_b32 %1, _Z6k_attn2P2-.Lpc_anchor_%=\n\ts_mov_b32 %2, _Z5k_out2P3-.Lpc_anchor_%=" : "=s"(pc), "=s"(oa), "=s"(oo));
        const unsigned long long ca = pc + (long long)oa, co = pc + (long long)oo;
        const long lim = (long)(co - ca) + 4096 - 128;
        const char* w2 = (const char*)ca; unsigned w2off = (wid & 3) * 8192 + lane * 128; if (lim < 0) w2off = 0; else if ((long)w2off > lim) w2off = (unsigned)lim;
        big_stream<true>(lds, (const char*)p.cKV + (size_t)pm * 256 * 1024, 1024, (const char*)p.wukB + pn * 512, (const char*)p.wuvB + pn * 512, acc,
                         [&](int) { store_f(p.ws + WS_KB, 1.f); }, warm, w2, w2off, self, wid, lane);
#pragma unroll
        for (int m = 0; m < 4; ++m)
#pragma unroll
            for (int n = 0; n < 2; ++n) { const int tb = pm * 8 + wr * 4 + m, fb = pn * 8 + wc * 2 + n, hd = fb >> 2, db = fb & 3;
                char* dst = p.ws + WS_VB + (size_t)((hd * NTB + tb) * 8 + db) * 1024 + lane * 16;
                st16(dst, pack8(acc[m][n], 0, 1.f)); st16(dst + 4096, pack8(acc[m][n], 8, 1.f)); }
    }
    STAMP(p.ws, 0, 1); STAMP(p.ws, 0, 2); STAMP(p.ws, 0, 3);
}

struct P2 { const u16 *xA, *wqA, *woA, *woB; char* ws; };

DI void exp16(const f32x16& s, unsigned (&pk)[8], float& t0, float& t1, unsigned& orv) {
#pragma unroll
    for (int i = 0; i < 8; ++i) {
        const float a = __builtin_amdgcn_exp2f(s[2 * i]), b = __builtin_amdgcn_exp2f(s[2 * i + 1]);
        t0 += a; t1 += b; pk[i] = cvtpk(a, b); orv |= pk[i];
    }
}
DI float max16(const f32x16& s) {
    float m = fmaxf(s[0], s[1]);
#pragma unroll
    for (int i = 2; i < 16; ++i) m = fmaxf(m, s[i]);
    return m;
}
DI void diag_mask(f32x16& s, int r, int h) {
#pragma unroll
    for (int i = 0; i < 16; ++i) { const int kr = (i & 3) + 8 * (i >> 2) + 4 * h; if (kr > r) s[i] = -__builtin_inff(); }
}
DI bf16x8 pfrag(const unsigned (&pk)[8], int b) { u32x4 w = {pk[b], pk[b + 1], pk[b + 2], pk[b + 3]}; return __builtin_bit_cast(bf16x8, w); }

#define XEXP(x) __builtin_amdgcn_exp2f(x)
DI void attnB(LAS char* lds, char* ws, int hd, int xb, int wid, int lane) {
    const int r = lane & 31, h = lane >> 5;
    const int tbq = 4 * xb + wid, NT = 2 * (xb + 1);
    const char* kbase = ws + WS_KB + (size_t)hd * NTB * 8192 + wid * 4096;
    const char* vbase = ws + WS_VB + (size_t)hd * NTB * 8192 + wid * 4096;
    const unsigned lds0 = (unsigned)(size_t)lds + wid * 4096, lane16 = lane * 16;
    auto piece = [&](const char* src, unsigned m0v, int i) { if (i == 0) glds16o<0>(src, lane16, m0v); else if (i == 1) glds16o<1024>(src, lane16, m0v); else if (i == 2) glds16o<2048>(src, lane16, m0v); else glds16o<3072>(src, lane16, m0v); };
    glds16x4(kbase, lane * 16, lds + wid * 4096); glds16x4(vbase, lane * 16, lds + 32768 + wid * 4096); glds16x4(kbase + 16384, lane * 16, lds + 16384 + wid * 4096);
    bf16x8 qf[8];
#pragma unroll
    for (int s = 0; s < 8; ++s) qf[s] = *(const bf16x8*)(ws + WS_QB + (size_t)((hd * NTB + tbq) * 8 + s) * 1024 + lane * 16);
    f32x16 o[4], negm, S0, S1; float l = 0.f, t0, t1;
    bf16x8 fr[8]; unsigned pk[8], pn[4];
#pragma unroll
    for (int d = 0; d < 4; ++d) o[d] = f32x16{};
    LAS const char* lbase = lds + lane * 16;
    auto kaddr = [&](int j) { return lbase + ((j >> 1) & 1) * 16384 + (j & 1) * 8192; };
    auto vaddr = [&](int j) { return lbase + 32768 + ((j >> 1) & 1) * 16384 + (j & 1) * 8192; };
    auto slotA = [&](auto ST, f32x16& Sc, f32x16& Sn, int j, const char* src, unsigned m0v) {
        LAS const char* va = vaddr(j);
        float ea = 0.f, eb = 0.f;
#pragma unroll
        for (int i = 0; i < 4; ++i) pk[i] = pn[i];
#pragma unroll
        for (int i = 0; i < 8; ++i) {
            Sn = mfma32(fr[i], qf[i], i ? Sn : negm);
            fr[i] = lds_b128(va + i * 1024);
            if (!(i & 1)) { ea = XEXP(Sc[8 + i]); eb = XEXP(Sc[9 + i]); PIN(ea); PIN(eb); if (ST.value) piece(src, m0v, i >> 1); }
            else { pk[4 + (i >> 1)] = cvtpk(ea, eb); t0 += ea; t1 += eb; PIN(t0); PIN(t1); }
            SBAR();
        }
        float ts = t0 + t1;
        if (__builtin_expect(__any(!(ts < 1.f)), 0)) {
            const float delta = fmaxf(swap32_max(max16(Sc)) + SM_BIAS, 0.f), alpha = __builtin_amdgcn_exp2f(-delta);
            l *= alpha;
#pragma unroll
            for (int d = 0; d < 4; ++d) o[d] *= alpha;
            negm -= delta; Sn -= delta; t0 = 0.f; t1 = 0.f;
#pragma unroll
            for (int i = 0; i < 8; ++i) { const float a = __builtin_amdgcn_exp2f(Sc[2 * i] - delta), b = __builtin_amdgcn_exp2f(Sc[2 * i + 1] - delta); pk[i] = cvtpk(a, b); t0 += a; t1 += b; }
            ts = t0 + t1;
        }
        l += ts;
        if (j + 1 == tbq) diag_mask(Sn, r, h);
    };
    auto slotB = [&](auto ST, f32x16& Sn, int j, const char* src, unsigned m0v) {
        LAS const char* ka = kaddr(j + 2);
        float ea = 0.f, eb = 0.f;
        t0 = 0.f; t1 = 0.f;
#pragma unroll
        for (int i = 0; i < 8; ++i) {
            o[i & 3] = mfma32(fr[i], pfrag(pk, 4 * (i >> 2)), o[i & 3]);
            fr[i] = lds_b128(ka + i * 1024);
            if (!(i & 1)) { if (ST.value) piece(src, m0v, i >> 1); ea = XEXP(Sn[i]); eb = XEXP(Sn[i + 1]); PIN(ea); PIN(eb); }
            else { pn[i >> 1] = cvtpk(ea, eb); t0 += ea; t1 += eb; PIN(pn[i >> 1]); PIN(t0); PIN(t1); }
            SBAR();
        }
    };
    using TT = std::integral_constant<bool, true>; using FF = std::integral_constant<bool, false>;
    WAIT_BAR(0);
    {
        S0 = f32x16{};
#pragma unroll
        for (int s = 0; s < 8; ++s) fr[s] = lds_b128(lbase + s * 1024);
#pragma unroll
        for (int s = 0; s < 8; ++s) S0 = mfma32(fr[s], qf[s], S0);
#pragma unroll
        for (int s = 0; s < 8; ++s) fr[s] = lds_b128(lbase + 8192 + s * 1024);
        if (tbq == 0) diag_mask(S0, r, h);
        const float nm = -(swap32_max(max16(S0)) + SM_BIAS);
#pragma unroll
        for (int i = 0; i < 16; ++i) negm[i] = nm;
        S0 += nm;
        t0 = 0.f; t1 = 0.f;
#pragma unroll
        for (int i = 0; i < 4; ++i) { const float a = __builtin_amdgcn_exp2f(S0[2 * i]), b = __builtin_amdgcn_exp2f(S0[2 * i + 1]); pn[i] = cvtpk(a, b); t0 += a; t1 += b; }
    }
    for (int j = 0;; j += 2) {
        const int t = j >> 1;
        WAIT_BAR(0);
#define BPRIO_MASK 3
        if ((t & BPRIO_MASK) != BPRIO_MASK || BPRIO_MASK == 0) __builtin_amdgcn_s_setprio(2); else __builtin_amdgcn_s_setprio(0);
        const int tk = t + 2 < NT ? t + 2 : NT - 1, tv = t + 1 < NT ? t + 1 : NT - 1;
        slotA(TT{}, S0, S1, j, kbase + (size_t)tk * 16384, lds0 + (t & 1) * 16384);  slotB(TT{}, S1, j, vbase + (size_t)tv * 16384, lds0 + 32768 + ((t + 1) & 1) * 16384);
        if (j == tbq) break;
        slotA(FF{}, S1, S0, j + 1, nullptr, 0u); slotB(FF{}, S0, j + 1, nullptr, 0u);
        if (j + 1 == tbq) break;
    }
    if (wid < 2) WAIT_BAR(0);
    asm volatile("s_waitcnt vmcnt(0)" ::: "memory");
    const float inv = 1.f / swap32_add(l);
    char* dst = ws + WS_CTX + (size_t)(tbq * 256 + hd * 8) * 1024 + lane * 16;
#pragma unroll
    for (int d = 0; d < 4; ++d) { st16(dst + (2 * d) * 1024, pack8(o[d], 0, inv)); st16(dst + (2 * d + 1) * 1024, pack8(o[d], 8, inv)); }
}

DI void projA(LAS char* lds, const P2& p, int hg, int tg, bf16x8 (&qf)[2][2], int wid, int lane) {
    constexpr int STG = 24576, NST = 16;
    const int r = lane & 31, h = lane >> 5;
    const char* xsrc = (const char*)p.xA + (size_t)tg * 64 * 2048;
    unsigned voX[2], voW[4];
#pragma unroll
    for (int i = 0; i < 2; ++i) { const int row = (wid * 2 + i) * 8 + (lane >> 3), ch = (lane & 7) ^ ((row >> 1) & 7); voX[i] = (unsigned)row * 2048u + ch * 16; }
#pragma unroll
    for (int i = 0; i < 4; ++i) { const int k = ((wid & 1) * 4 + i) * 8 + (lane >> 3), slot = lane & 7;
        voW[i] = (unsigned)k * 2048u + (128 * hg + 64 * (wid >> 1) + 32 * ((slot >> 2) ^ ((k >> 1) & 1)) + 8 * (slot & 3)) * 2; }
    auto stage1 = [&](int slot, int t, int i) {
        LAS char* base = lds + slot * STG;
        if (i < 2) glds16(xsrc + (size_t)t * 128, voX[i], base + (wid * 2 + i) * 1024);
        else glds16((const char*)p.wqA + (size_t)t * 64 * 2048, voW[i - 2], base + 8192 + (wid >> 1) * 8192 + ((wid & 1) * 4 + i - 2) * 1024);
    };
    int aoff[4];
#pragma unroll
    for (int s = 0; s < 4; ++s) aoff[s] = r * 128 + (((2 * s + h) ^ ((r >> 1) & 7)) << 4);
    const int blk = (lane >> 4) & 1, q = (lane & 15) >> 2, pq = lane & 3;
    const int boff = 8192 + (wid >> 1) * 8192 + (8 * h + q) * 128 + 64 * ((wid & 1) ^ (q >> 1)) + 32 * blk + 8 * pq;
    f32x16 acc[2]; acc[0] = f32x16{}; acc[1] = f32x16{};
    bf16x8 wf[4], a0[4], a1[4];
    auto rd = [&](int slot, int s) { LAS const char* sa = lds + slot * STG;
        wf[s] = cat8(lds_tr(sa + boff + s * 2048), lds_tr(sa + boff + s * 2048 + 512)); a0[s] = lds_b128(sa + aoff[s]); a1[s] = lds_b128(sa + aoff[s] + 4096); };
#pragma unroll
    for (int t = 0; t < 3; ++t)
#pragma unroll
        for (int i = 0; i < 6; ++i) stage1(t, t, i);
    WAIT_BAR(12);
    rd(0, 0); rd(0, 1);
    int slot = 0;
    for (int t = 0; t < NST; ++t) {
        int sn = slot + 1; if (sn == 3) sn = 0;
        rd(slot, 2); acc[0] = mfma32(wf[0], a0[0], acc[0]); acc[1] = mfma32(wf[0], a1[0], acc[1]); SBAR();
        rd(slot, 3); acc[0] = mfma32(wf[1], a0[1], acc[0]); acc[1] = mfma32(wf[1], a1[1], acc[1]); SBAR();
        if (t + 2 < NST) { WAIT_BAR(6); } else { WAIT_BAR(0); }
        rd(sn, 0); acc[0] = mfma32(wf[2], a0[2], acc[0]); acc[1] = mfma32(wf[2], a1[2], acc[1]); SBAR();
        if (t + 3 < NST) { stage1(slot, t + 3, 0); stage1(slot, t + 3, 1); stage1(slot, t + 3, 2); }
        rd(sn, 1); acc[0] = mfma32(wf[3], a0[3], acc[0]); acc[1] = mfma32(wf[3], a1[3], acc[1]); SBAR();
        if (t + 3 < NST) { stage1(slot, t + 3, 3); stage1(slot, t + 3, 4); stage1(slot, t + 3, 5); }
        slot = sn;
    }
#pragma unroll
    for (int u = 0; u < 2; ++u)
#pragma unroll
        for (int s = 0; s < 2; ++s) qf[u][s] = __builtin_bit_cast(bf16x8, pack8(acc[u], 8 * s, CA));
    WAIT_BAR(0);
}
DI void attnA(LAS char* lds, const P2& p, int hg, int tg, int wid, int lane) {
    char* ws = p.ws;
    const int hd = 4 * hg + wid, tb0 = 2 * tg;
    bf16x8 qf[2][2];
    __builtin_amdgcn_s_setprio(3);
    projA(lds, p, hg, tg, qf, wid, lane);
    STAMP(ws, 1, 1);
    __builtin_amdgcn_s_setprio(1);
    STAMP(ws, 1, 2);
    const char* kvsrc = ws + ((wid & 2) ? WS_VA : WS_KA) + (wid & 1) * 4096;
    auto stage = [&](int slot, int t) { glds16x4(kvsrc + (size_t)t * 8192, lane * 16, lds + slot * 16384 + wid * 4096); };
    stage(0, 0); stage(1, 1);
    f32x16 o[2], negm[2], Sa[2], Sb[2]; float l[2] = {0.f, 0.f};
    o[0] = f32x16{}; o[1] = f32x16{};
    bf16x8 kf[2], vf[2]; unsigned pp[2][8];
    LAS const char* lbase = lds + lane * 16;
    auto step = [&](f32x16 (&Sc)[2], f32x16 (&Sn)[2], LAS const char* ka, LAS const char* va) {
        unsigned pn[2][8]; float t[2][2] = {{0.f, 0.f}, {0.f, 0.f}}, e[2][4];
#pragma unroll
        for (int i = 0; i < 9; ++i) {
            if (i < 8) {
                const int u = i & 1, s = (i >> 1) & 1;
                if (i < 4) Sn[u] = mfma32(kf[s], qf[u][s], s ? Sn[u] : negm[u]);
                else o[u] = mfma32(vf[s], pfrag(pp[u], 4 * s), o[u]);
                if (i == 3) { kf[0] = lds_b128(ka); kf[1] = lds_b128(ka + 1024); }
                if (i == 7) { vf[0] = lds_b128(va); vf[1] = lds_b128(va + 1024); }
#pragma unroll
                for (int k = 0; k < 4; ++k) { e[i & 1][k] = __builtin_amdgcn_exp2f(Sc[i >> 2][(i & 3) * 4 + k]); PIN(e[i & 1][k]); }
            }
            if (i > 0) {
                const int g = i - 1, uu = g >> 2, w = (g & 3) * 2; const float* x = e[g & 1];
                pn[uu][w] = cvtpk(x[0], x[1]); pn[uu][w + 1] = cvtpk(x[2], x[3]);
                t[uu][0] += x[0]; t[uu][1] += x[1]; t[uu][0] += x[2]; t[uu][1] += x[3]; PIN(t[uu][0]); PIN(t[uu][1]); PIN(pn[uu][w]); PIN(pn[uu][w + 1]);
            }
            if (i < 8) SBAR();
        }
        float ts[2] = {t[0][0] + t[0][1], t[1][0] + t[1][1]};
        if (__builtin_expect(__any(!(ts[0] + ts[1] < 1.f)), 0)) {
#pragma unroll
            for (int u = 0; u < 2; ++u) {
                const float dl = fmaxf(swap32_max(max16(Sc[u])) + SM_BIAS, 0.f), al = __builtin_amdgcn_exp2f(-dl);
                l[u] *= al; o[u] *= al; negm[u] -= dl; Sn[u] -= dl; float a0 = 0.f, a1 = 0.f;
#pragma unroll
                for (int i = 0; i < 8; ++i) { const float a = __builtin_amdgcn_exp2f(Sc[u][2 * i] - dl), b = __builtin_amdgcn_exp2f(Sc[u][2 * i + 1] - dl); pn[u][i] = cvtpk(a, b); a0 += a; a1 += b; }
                ts[u] = a0 + a1;
            }
        }
#pragma unroll
        for (int u = 0; u < 2; ++u) { l[u] += ts[u];
#pragma unroll
            for (int i = 0; i < 8; ++i) pp[u][i] = pn[u][i]; }
    };
    WAIT_BAR(4);
    {
        kf[0] = lds_b128(lbase); kf[1] = lds_b128(lbase + 1024);
#pragma unroll
        for (int u = 0; u < 2; ++u) {
            Sa[u] = mfma32(kf[0], qf[u][0], f32x16{}); Sa[u] = mfma32(kf[1], qf[u][1], Sa[u]);
            const float nm = -(swap32_max(max16(Sa[u])) + SM_BIAS);
#pragma unroll
            for (int i = 0; i < 16; ++i) negm[u][i] = nm;
            Sa[u] += nm;
#pragma unroll
            for (int i = 0; i < 8; ++i) pp[u][i] = 0u;
        }
        kf[0] = lds_b128(lbase + 2048); kf[1] = lds_b128(lbase + 2048 + 1024);
        vf[0] = kf[0]; vf[1] = kf[1];
    }
    int slot = 0;
    for (int t = 0; t < 16; ++t) {
        WAIT_BAR(0);
#define APRIO_T 99
        if (t == APRIO_T) __builtin_amdgcn_s_setprio(3);
        int sn = slot + 1; if (sn == 3) sn = 0; int sp = sn + 1; if (sp == 3) sp = 0;
        if (t + 2 < 16) stage(sp, t + 2);
        if (t == 2) { const int gw = (hg * 32 + tg) * 4 + wid; l2pf((const char*)p.woB + (size_t)gw * 8192, lane * 128, lds + 49152 + wid * 256);
                      l2pf((const char*)p.woA + (size_t)gw * 2048, (lane & 15) * 128, lds + 49152 + wid * 256); }
        LAS const char* cur = lbase + slot * 16384; LAS const char* nxt = lbase + sn * 16384;
        step(Sa, Sb, cur + 2 * 2048, cur + 8192);
        step(Sb, Sa, cur + 3 * 2048, cur + 8192 + 2048);
        step(Sa, Sb, nxt, cur + 8192 + 2 * 2048);
        step(Sb, Sa, nxt + 2048, cur + 8192 + 3 * 2048);
        slot = sn;
    }
#pragma unroll
    for (int s = 0; s < 2; ++s)
#pragma unroll
        for (int u = 0; u < 2; ++u) o[u] = mfma32(vf[s], pfrag(pp[u], 4 * s), o[u]);
#pragma unroll
    for (int u = 0; u < 2; ++u) {
        const float inv = 1.f / swap32_add(l[u]);
        char* dst = ws + WS_OA + (size_t)((tb0 + u) * 64 + hd * 2) * 1024 + lane * 16;
        st16(dst, pack8(o[u], 0, inv)); st16(dst + 1024, pack8(o[u], 8, inv));
    }
}

__global__ __launch_bounds__(256, 2) void k_attn(P2 p) {
    extern __shared__ __attribute__((aligned(16))) char smem[];
    LAS char* lds = (LAS char*)smem;
    const int tid = threadIdx.x, wid = __builtin_amdgcn_readfirstlane(tid >> 6), lane = tid & 63;
    const int bid = blockIdx.x;
    const int xcd = bid & 7, idx = bid >> 3,
        kind = (idx ^ (idx >> 5)) & 1,
        it = (idx >> 1) & 15 | (idx >> 5) << 4;
    STAMP(p.ws, 1, 0);
    if (kind == 0) { attnA(lds, p, xcd, it, wid, lane); }
    else {
        const int hd = xcd + 8 * (it >> 3), x = it & 7;
        attnB(lds, p.ws, hd, 15 - x, wid, lane);
        WAIT_BAR(0);
        STAMP(p.ws, 1, 1);
        attnB(lds, p.ws, hd, x, wid, lane); STAMP(p.ws, 1, 2); }
    STAMP(p.ws, 1, 3);
}

struct P3 { const u16 *woA, *woB; char* ws; u16* out; };

constexpr int NT1 = 24;
DI void out_store(u16* out, int tb0, int col0, const f32x16 (&acc)[2][2], int wid, int lane) {
    const int wm = wid >> 1, wn = wid & 1, r = lane & 31, h = lane >> 5;
#pragma unroll
    for (int m = 0; m < 2; ++m) {
        char* dst = (char*)out + (size_t)((tb0 + 2 * wm + m) * 32 + r) * 4096 + (col0 + 64 * wn + 8 * h) * 2;
#pragma unroll
        for (int c = 0; c < 2; ++c) { *(u32x4*)(dst + c * 64) = pack8(acc[m][c], 0, 1.f); *(u32x4*)(dst + c * 64 + 32) = pack8(acc[m][c], 8, 1.f); }
    }
}

__global__ __launch_bounds__(512, 2) void k_out(P3 p) {
    extern __shared__ __attribute__((aligned(16))) char smem[];
    LAS char* lds = (LAS char*)smem;
    constexpr int STG = 32768, nt = 40;
    const int tid = threadIdx.x, wid = __builtin_amdgcn_readfirstlane(tid >> 6), lane = tid & 63;
    const int bid = blockIdx.x, xg = bid & 7, role = (xg >> 2) ^ 1, ti = bid >> 3;
    const int pm = 8 * ((xg >> 1) & 1) + (ti >> 2), pn = 4 * (xg & 1) + (ti & 3), tb0 = pm * 4, tau = pm * 8 + pn;
    STAMP(p.ws, 2, 0);
    if (wid >= 4) {
        const int lw = wid - 4;
        const char* a0 = p.ws + WS_CTX + (size_t)((tb0 + lw) * 256 + (role ? 4 * (64 - NT1) : 0)) * 1024;
        const char* w0 = (const char*)p.woB + pn * 256 + (size_t)(role ? 64 - NT1 : 0) * 64 * 2048;
        const char* a1 = p.ws + WS_OA + (size_t)((tb0 + lw) * 64) * 1024 - (size_t)NT1 * 4096;
        const char* w1 = (const char*)p.woA + pn * 256 - (size_t)NT1 * 64 * 2048;
        const int tsw = role ? NT1 : nt;
        unsigned voW[4];
#pragma unroll
        for (int i = 0; i < 4; ++i) { const int pc = 4 * lw + i, g = pc >> 3, k = (pc & 7) * 8 + (lane >> 3), slot = lane & 7;
            const int col = 64 * g + 32 * ((slot >> 2) ^ ((k >> 1) & 1)) + 8 * (slot & 3); voW[i] = (unsigned)k * 2048u + col * 2; }
        auto stage = [&](int t) {
            LAS char* base = lds + (t & 3) * STG;
            const char* as = (t < tsw ? a0 : a1) + (size_t)t * 4096; const char* wsd = (t < tsw ? w0 : w1) + (size_t)t * (64 * 2048);
            glds16x4(as, lane * 16, base + lw * 4096);
#pragma unroll
            for (int i = 0; i < 4; ++i) glds16(wsd, voW[i], base + 16384 + (4 * lw + i) * 1024);
        };
#pragma unroll
        for (int t = 0; t < 4; ++t) stage(t);
        WAIT_BAR(24);
        for (int t = 0; t < nt; ++t) {
            if (t + 3 < nt) { WAIT_BAR(16); } else if (t + 2 < nt) { WAIT_BAR(8); } else { WAIT_BAR(0); }
            if (t + 4 < nt) stage(t + 4);
        }
    } else {
        const int wm = wid >> 1, wn = wid & 1, h = lane >> 5;
        const int blk = (lane >> 4) & 1, q = (lane & 15) >> 2, pq = lane & 3, pp = ((pq & 1) << 1) | (pq >> 1);
        GAS unsigned* flag = (GAS unsigned*)(p.ws + WS_FLAG) + tau * 16;
        const __amdgpu_buffer_rsrc_t rs = __builtin_amdgcn_make_buffer_rsrc(p.ws + WS_PART + (size_t)tau * 65536, 0, 65536, 0x00020000);
        int boff[2];
#pragma unroll
        for (int c = 0; c < 2; ++c) boff[c] = 16384 + wn * 8192 + (4 * h + q) * 128 + 64 * (c ^ (q >> 1)) + 32 * blk + 8 * pp;
        const int aoff = (2 * wm * 4) * 1024 + lane * 16;
        f32x16 acc[2][2];
#pragma unroll
        for (int m = 0; m < 2; ++m) { acc[m][0] = f32x16{}; acc[m][1] = f32x16{}; }
        bf16x8 af[4][2], wf[4][2];
        auto rd = [&](int slot, int s, int i) {
            LAS const char* sa = lds + slot * STG;
            if (i & 1) af[s][i >> 1] = lds_b128(sa + aoff + ((i >> 1) * 4 + s) * 1024);
            else wf[s][i >> 1] = cat8(lds_tr(sa + boff[i >> 1] + s * 2048), lds_tr(sa + boff[i >> 1] + s * 2048 + 1024));
        };
        auto kstep = [&](int s, int nslot) {
#pragma unroll
            for (int i = 0; i < 4; ++i) {
                const int m = i >> 1, c = i & 1;
                acc[m][c] = mfma32(wf[s][c], af[s][m], acc[m][c]);
                rd(nslot, (s + 2) & 3, i);
                SBAR();
            }
        };
        auto tiles = [&](int t0, int t1) {
#pragma unroll 1
            for (int t = t0; t < t1; ++t) {
                kstep(0, t & 3); kstep(1, t & 3);
                asm volatile("s_waitcnt lgkmcnt(0)\n\ts_barrier" ::: "memory");
                kstep(2, (t + 1) & 3); kstep(3, (t + 1) & 3);
            }
        };
        WAIT_BAR(0);
#pragma unroll
        for (int i = 0; i < 4; ++i) { rd(0, 0, i); rd(0, 1, i); }
        if (role == 1) {
            tiles(0, NT1);
#pragma unroll
            for (int m = 0; m < 2; ++m)
#pragma unroll
                for (int c = 0; c < 2; ++c) {
#pragma unroll
                    for (int i = 0; i < 4; ++i) { u32x4 v;
#pragma unroll
                        for (int j = 0; j < 4; ++j) v[j] = __float_as_uint(acc[m][c][4 * i + j]);
                        __builtin_amdgcn_raw_buffer_store_b128(v, rs, wid * 16384 + ((m * 2 + c) * 4 + i) * 1024 + lane * 16, 0, 16); }
                    acc[m][c] = f32x16{}; }
            tiles(NT1, NT1 + 4);
            asm volatile("s_waitcnt vmcnt(0)" ::: "memory");
            tiles(NT1 + 4, NT1 + 5);
            if (tid == 0) __hip_atomic_store(flag, 1u, __ATOMIC_RELAXED, __HIP_MEMORY_SCOPE_AGENT);
            STAMP(p.ws, 2, 1);
            tiles(NT1 + 5, nt);
            STAMP(p.ws, 2, 2);
            out_store(p.out, tb0, pn * 128, acc, wid, lane);
        } else {
            tiles(0, nt - 5);
            while ((unsigned)__builtin_amdgcn_readfirstlane(__hip_atomic_load(flag, __ATOMIC_RELAXED, __HIP_MEMORY_SCOPE_AGENT)) != 1u) __builtin_amdgcn_s_sleep(2);
            __builtin_amdgcn_fence(__ATOMIC_ACQUIRE, "wavefront");
            u32x4 pv[16];
#pragma unroll
            for (int i = 0; i < 16; ++i) pv[i] = __builtin_amdgcn_raw_buffer_load_b128(rs, wid * 16384 + i * 1024 + lane * 16, 0, 16);
            STAMP(p.ws, 2, 1);
            tiles(nt - 5, nt);
#pragma unroll
            for (int i = 0; i < 16; ++i)
#pragma unroll
                for (int j = 0; j < 4; ++j) acc[i >> 3][(i >> 2) & 1][4 * (i & 3) + j] += __uint_as_float(pv[i][j]);
            STAMP(p.ws, 2, 2);
            out_store(p.out, tb0, 1024 + pn * 128, acc, wid, lane);
        }
        STAMP(p.ws, 2, 3);
    }
}

#define BENCH_MARK(s)
extern "C" void kernel_launch(void* const* d_in, const int* in_sizes, int n_in, void* d_out, int out_size, void* d_ws, size_t ws_size,
                              hipStream_t stream) {
    (void)in_sizes; (void)n_in; (void)out_size; (void)ws_size;
    (void)hipFuncSetAttribute((const void*)k_proj, hipFuncAttributeMaxDynamicSharedMemorySize, 133120);
    (void)hipFuncSetAttribute((const void*)k_attn, hipFuncAttributeMaxDynamicSharedMemorySize, 73728);
    (void)hipFuncSetAttribute((const void*)k_out, hipFuncAttributeMaxDynamicSharedMemorySize, 131072);
    P1 p1; p1.wqA = (const u16*)d_in[1]; p1.xA = (const u16*)d_in[0]; p1.wkA = (const u16*)d_in[2]; p1.wvA = (const u16*)d_in[3];
    p1.xB = (const u16*)d_in[5]; p1.cKV = (const u16*)d_in[6]; p1.wqB = (const u16*)d_in[7]; p1.wukB = (const u16*)d_in[8]; p1.wuvB = (const u16*)d_in[9];
    p1.ws = (char*)d_ws;
    BENCH_MARK(stream);
    k_proj<<<256, 512, 133120, stream>>>(p1);
    BENCH_MARK(stream);
    P2 p2; p2.woA = (const u16*)d_in[4]; p2.woB = (const u16*)d_in[10]; p2.xA = (const u16*)d_in[0]; p2.wqA = (const u16*)d_in[1]; p2.ws = (char*)d_ws;
    k_attn<<<512, 256, 73728, stream>>>(p2);
    BENCH_MARK(stream);
    P3 p3; p3.woA = (const u16*)d_in[4]; p3.woB = (const u16*)d_in[10]; p3.ws = (char*)d_ws; p3.out = (u16*)d_out;
    k_out<<<256, 512, 131072, stream>>>(p3);
    BENCH_MARK(stream);
}
```

```cpp
#include <hip/hip_runtime.h>
#include <hip/hip_bf16.h>
#include <type_traits>

typedef unsigned short u16;
typedef short bf16x8 __attribute__((ext_vector_type(8)));
typedef short bf16x4 __attribute__((ext_vector_type(4)));
typedef float f32x16 __attribute__((ext_vector_type(16)));
typedef float f32x4 __attribute__((ext_vector_type(4)));
typedef float f32x2 __attribute__((ext_vector_type(2)));
typedef __bf16 bf16x2_t __attribute__((ext_vector_type(2)));
typedef unsigned u32x4 __attribute__((ext_vector_type(4)));
#define LAS __attribute__((address_space(3)))
#define GAS __attribute__((address_space(1)))
#define DI __device__ __forceinline__

DI f32x16 mfma32(bf16x8 a, bf16x8 b, f32x16 c) { return __builtin_amdgcn_mfma_f32_32x32x16_bf16(a, b, c, 0, 0, 0); }
DI unsigned cvtpk(float lo, float hi) { f32x2 v = {lo, hi}; bf16x2_t b = __builtin_convertvector(v, bf16x2_t); return __builtin_bit_cast(unsigned, b); }
DI void glds16(const void* sbase, unsigned voff, LAS void* l) {
    unsigned sv; asm volatile("s_mov_b32 %0, m0\n\ts_mov_b32 m0, %3\n\ts_nop 0\n\tglobal_load_lds_dwordx4 %1, %2\n\ts_mov_b32 m0, %0"
                              : "=&s"(sv) : "v"(voff), "s"(sbase), "s"((unsigned)(size_t)l) : "memory"); }
template <int OFF> DI void glds16o(const void* sbase, unsigned voff, unsigned m0v) {
    asm volatile("s_mov_b32 m0, %2\n\ts_nop 0\n\tglobal_load_lds_dwordx4 %0, %1 offset:%3" :: "v"(voff), "s"(sbase), "s"(m0v), "i"(OFF) : "memory"); }
DI void l2pf(const void* sbase, unsigned voff, LAS void* scratch) {
    unsigned sv; asm volatile("s_mov_b32 %0, m0\n\ts_mov_b32 m0, %3\n\ts_nop 0\n\tglobal_load_lds_dword %1, %2\n\ts_mov_b32 m0, %0"
                              : "=&s"(sv) : "v"(voff), "s"(sbase), "s"((unsigned)(size_t)scratch) : "memory"); }
#define CODE_PF(SYM, LENEXPR, w, scratch) do { unsigned long long pc_; int o0_, o1_; \
    asm volatile("s_getpc_b64 %0\n.Lpcs_%=:\n\ts_mov_b32 %1, " SYM "-.Lpcs_%=\n\ts_mov_b32 %2, " LENEXPR : "=s"(pc_), "=s"(o0_), "=s"(o1_)); \
    int off_ = (w) * 8192 + lane * 128; if (off_ > o1_ - 128) off_ = o1_ - 128; if (off_ < 0) off_ = 0; \
    l2pf((const char*)(pc_ + (long long)o0_), (unsigned)off_, scratch); } while (0)
DI void glds16v(const void* vaddr, LAS void* l) {
    unsigned sv; asm volatile("s_mov_b32 %0, m0\n\ts_mov_b32 m0, %2\n\ts_nop 0\n\tglobal_load_lds_dwordx4 %1, off\n\ts_mov_b32 m0, %0"
                              : "=&s"(sv) : "v"(vaddr), "s"((unsigned)(size_t)l) : "memory"); }
DI void glds16x4(const void* sbase, unsigned voff, LAS void* l) {
    unsigned sv; asm volatile("s_mov_b32 %0, m0\n\ts_mov_b32 m0, %3\n\ts_nop 0\n\tglobal_load_lds_dwordx4 %1, %2\n\tglobal_load_lds_dwordx4 %1, %2 offset:1024\n\t"
                              "global_load_lds_dwordx4 %1, %2 offset:2048\n\tglobal_load_lds_dwordx4 %1, %2 offset:3072\n\ts_mov_b32 m0, %0"
                              : "=&s"(sv) : "v"(voff), "s"(sbase), "s"((unsigned)(size_t)l) : "memory"); }
DI void glds16x2(const void* sbase, unsigned voff, LAS void* l) {
    unsigned sv; asm volatile("s_mov_b32 %0, m0\n\ts_mov_b32 m0, %3\n\ts_nop 0\n\tglobal_load_lds_dwordx4 %1, %2\n\tglobal_load_lds_dwordx4 %1, %2 offset:1024\n\ts_mov_b32 m0, %0"
                              : "=&s"(sv) : "v"(voff), "s"(sbase), "s"((unsigned)(size_t)l) : "memory"); }
DI bf16x8 lds_b128(LAS const char* p) { return *(LAS const bf16x8*)p; }
DI bf16x4 lds_tr(LAS const char* p) { return __builtin_amdgcn_ds_read_tr16_b64_v4i16((LAS bf16x4*)p); }
DI bf16x8 cat8(bf16x4 a, bf16x4 b) { return (bf16x8){a[0], a[1], a[2], a[3], b[0], b[1], b[2], b[3]}; }
DI float swap32_max(float x) { auto rr = __builtin_amdgcn_permlane32_swap(__float_as_uint(x), __float_as_uint(x), false, false); return fmaxf(__uint_as_float(rr[0]), __uint_as_float(rr[1])); }
DI float swap32_add(float x) { auto rr = __builtin_amdgcn_permlane32_swap(__float_as_uint(x), __float_as_uint(x), false, false); return __uint_as_float(rr[0]) + __uint_as_float(rr[1]); }
#define WAIT_BAR(N) asm volatile("s_waitcnt vmcnt(" #N ") lgkmcnt(0)\n\ts_barrier" ::: "memory")
#define SBAR() __builtin_amdgcn_sched_barrier(0)
#define PIN(x) asm volatile("" : "+v"(x))
#define WAIT_BAR_I(N) asm volatile("s_waitcnt vmcnt(%0) lgkmcnt(0)\n\ts_barrier" :: "i"(N) : "memory")

DI void st16(void* p, u32x4 v) { *(u32x4*)p = v; }
DI u32x4 pack8(const f32x16& a, int b, float sc) {
    u32x4 w; w.x = cvtpk(a[b + 0] * sc, a[b + 1] * sc); w.y = cvtpk(a[b + 2] * sc, a[b + 3] * sc);
    w.z = cvtpk(a[b + 4] * sc, a[b + 5] * sc); w.w = cvtpk(a[b + 6] * sc, a[b + 7] * sc); return w;
}

constexpr int SEQ = 2048, NTB = SEQ / 32;
constexpr float LOG2E = 1.4426950408889634f;
constexpr float CA = 0.17677669529663687f * LOG2E;
constexpr float CB = 0.08838834764831845f * LOG2E;
constexpr float SM_BIAS = 64.f;

constexpr size_t WS_QB = 0, WS_KB = WS_QB + (16u << 20), WS_VB = WS_KB + (16u << 20), WS_QA = WS_VB + (16u << 20),
                 WS_KA = WS_QA + (4u << 20), WS_VA = WS_KA + (128u << 10), WS_CTX = WS_VA + (128u << 10), WS_OA = WS_CTX + (16u << 20),
                 WS_PART = WS_OA + (4u << 20), WS_FLAG = WS_PART + (8u << 20), WS_END = WS_FLAG + 129 * 64;

#define STAMP(ws, kn, i)
struct P1 { const u16 *xB, *cKV, *wqB, *wukB, *wuvB, *xA, *wkA, *wvA, *wqA; char* ws; };

template <bool KV, class Mid>
DI void big_stream(LAS char* lds, const char* Ab, unsigned lda_b, const char* W0, const char* W1, f32x16 (&acc)[4][2], Mid mid, const char* warm, const char* warm2, unsigned w2off, int wid, int lane) {
    constexpr unsigned ldw_b = 8192;
    const int wr = wid >> 2, wc = wid & 3, r = lane & 31, h = lane >> 5;
    unsigned voA[4], voW[2];
#pragma unroll
    for (int i = 0; i < 4; ++i) { const int row = (wid * 4 + i) * 8 + (lane >> 3), ch = (lane & 7) ^ ((row >> 1) & 7); voA[i] = (unsigned)row * lda_b + ch * 16; }
#pragma unroll
    for (int i = 0; i < 2; ++i) {
        const int k = ((wid & 1) * 2 + i) * 8 + (lane >> 3), slot = lane & 7;
        const int col = 64 * (wid >> 1) + 32 * ((slot >> 2) ^ ((k >> 1) & 1)) + 8 * (slot & 3);
        voW[i] = (unsigned)k * ldw_b + col * 2;
    }
    int aoff[4], boff[2];
#pragma unroll
    for (int s = 0; s < 4; ++s) aoff[s] = (128 * wr + r) * 128 + (((2 * s + h) ^ ((r >> 1) & 7)) << 4);
    { const int blk = (lane >> 4) & 1, q = (lane & 15) >> 2, p = lane & 3;
#pragma unroll
      for (int n = 0; n < 2; ++n) boff[n] = 65536 + wc * 4096 + (8 * h + q) * 128 + 64 * (n ^ (q >> 1)) + 32 * blk + 8 * p; }
#pragma unroll
    for (int m = 0; m < 4; ++m) { acc[m][0] = f32x16{}; acc[m][1] = f32x16{}; }
    auto stA = [&](int pr, int i) { glds16(Ab + (size_t)(KV ? (pr & 7) : pr) * 128, voA[i], lds + (pr & 1) * 32768 + (wid * 4 + i) * 1024); };
    auto stW = [&](int t, int i) { glds16(((KV && t >= 16) ? W1 : W0) + (size_t)(KV ? (t & 15) : t) * 32 * ldw_b, voW[i], lds + 65536 + (t & 3) * 16384 + (wid >> 1) * 4096 + ((wid & 1) * 2 + i) * 1024); };
    bf16x8 af[2][4], wf[2][2];
    auto rd = [&](int rs, int t, int par, int s, int i) {
        if (i < 2) { LAS const char* sw = lds + (t & 3) * 16384; wf[rs][i] = cat8(lds_tr(sw + boff[i] + s * 2048), lds_tr(sw + boff[i] + s * 2048 + 512)); }
        else af[rs][i - 2] = lds_b128(lds + ((t >> 1) & 1) * 32768 + aoff[2 * par + s] + (i - 2) * 4096);
    };
    auto ks0 = [&](auto OT, int t, int par) {
#pragma unroll
        for (int i = 0; i < 8; ++i) { const int m = i >> 1, n = i & 1;
            acc[m][n] = OT.value ? mfma32(wf[0][n], af[0][m], acc[m][n]) : mfma32(af[0][m], wf[0][n], acc[m][n]);
            if (i < 6) rd(1, t, par, 1, i);
            SBAR(); }
    };
    auto ks1 = [&](auto OT, int t, int par, bool ld) {
#pragma unroll
        for (int i = 0; i < 8; ++i) { const int m = i >> 1, n = i & 1;
            acc[m][n] = OT.value ? mfma32(wf[1][n], af[1][m], acc[m][n]) : mfma32(af[1][m], wf[1][n], acc[m][n]);
            if (i < 6) rd(0, t + 1, par ^ 1, 0, i);
            if (ld) { if (par) { if (i >= 1 && i <= 4) stA((t + 3) >> 1, i - 1); if (i >= 5 && i <= 6) stW(t + 4, i - 5); } else if (i == 2 || i == 5) stW(t + 4, i == 5); }
            SBAR(); }
    };
    using TT = std::integral_constant<bool, true>; using FF = std::integral_constant<bool, false>;
#pragma unroll
    for (int pr = 0; pr < 2; ++pr) {
#pragma unroll
        for (int i = 0; i < 4; ++i) stA(pr, i);
        stW(2 * pr, 0); stW(2 * pr, 1); stW(2 * pr + 1, 0); stW(2 * pr + 1, 1); }
    WAIT_BAR(10);
#pragma unroll
    for (int i = 0; i < 6; ++i) rd(0, 0, 0, 0, i);
#pragma unroll 1
    for (int t = 0; t < 12; t += 2) {
        ks0(TT{}, t, 0);     WAIT_BAR(8); ks1(TT{}, t, 0, true);
        ks0(TT{}, t + 1, 1); WAIT_BAR(4); ks1(TT{}, t + 1, 1, true);
    }
    ks0(TT{}, 12, 0); WAIT_BAR(8);
    l2pf(warm, (lane < 24 ? lane : 0) * 128, lds + 131072 + wid * 256); l2pf(warm2, w2off, lds + 131072 + wid * 256);
    ks1(TT{}, 12, 0, true);
    ks0(TT{}, 13, 1); WAIT_BAR(6);  ks1(TT{}, 13, 1, true);
    ks0(TT{}, 14, 0); WAIT_BAR(10); ks1(TT{}, 14, 0, true);
    ks0(TT{}, 15, 1); WAIT_BAR(4);  ks1(TT{}, 15, 1, true);
    if constexpr (!KV) {
#pragma unroll 1
        for (int t = 16; t < 28; t += 2) {
            ks0(TT{}, t, 0);     WAIT_BAR(8); ks1(TT{}, t, 0, true);
            ks0(TT{}, t + 1, 1); WAIT_BAR(4); ks1(TT{}, t + 1, 1, true);
        }
        ks0(TT{}, 28, 0); WAIT_BAR(8);  ks1(TT{}, 28, 0, false);
        ks0(TT{}, 29, 1); WAIT_BAR(2);  mid(0); mid(1); ks1(TT{}, 29, 1, false);
        ks0(TT{}, 30, 0); WAIT_BAR(16); ks1(TT{}, 30, 0, false);
        ks0(TT{}, 31, 1); ks1(TT{}, 30, 0, false);
    } else {
        mid(0);
#pragma unroll
        for (int m = 0; m < 4; ++m) { acc[m][0] = f32x16{}; acc[m][1] = f32x16{}; }
        ks0(FF{}, 16, 0); WAIT_BAR(24); ks1(FF{}, 16, 0, true);
        ks0(FF{}, 17, 1); WAIT_BAR(20); ks1(FF{}, 17, 1, true);
        ks0(FF{}, 18, 0); WAIT_BAR(24); ks1(FF{}, 18, 0, true);
        ks0(FF{}, 19, 1); WAIT_BAR(4);  ks1(FF{}, 19, 1, true);
#pragma unroll 1
        for (int t = 20; t < 28; t += 2) {
            ks0(FF{}, t, 0);     WAIT_BAR(8); ks1(FF{}, t, 0, true);
            ks0(FF{}, t + 1, 1); WAIT_BAR(4); ks1(FF{}, t + 1, 1, true);
        }
        ks0(FF{}, 28, 0); WAIT_BAR(8); ks1(FF{}, 28, 0, false);
        ks0(FF{}, 29, 1); WAIT_BAR(2); ks1(FF{}, 29, 1, false);
        ks0(FF{}, 30, 0); WAIT_BAR(0); ks1(FF{}, 30, 0, false);
        ks0(FF{}, 31, 1); ks1(FF{}, 30, 0, false);
    }
}
constexpr int KVP1 = 65536;

struct KvUnit {
    bf16x8 xf[8];
    DI void issue(int part, LAS char* lds, const P1& p, int tb, int isv, int wid, int lane) {
        const char* wsrc = (const char*)(isv ? p.wvA : p.wkA) + wid * 8192;
        glds16x4(wsrc + part * 4096, lane * 16, lds + part * KVP1 + wid * 4096);
        if (part) {
#pragma unroll
            for (int s = 0; s < 8; ++s) xf[s] = *(const bf16x8*)((const char*)p.xA + (size_t)(32 * tb + (lane & 31)) * 2048 + (128 * wid + 16 * s + 8 * (lane >> 5)) * 2);
        }
    }
    template <class St>
    DI void finish(LAS char* lds, const P1& p, int tb, int isv, int wid, int lane, St st) {
        const int h = lane >> 5, blk = (lane >> 4) & 1, q = (lane & 15) >> 2, pq = lane & 3;
        st(0); st(1); SBAR();
        asm volatile("s_waitcnt vmcnt(4)" ::: "memory");
        f32x16 ak = f32x16{};
#pragma unroll
        for (int s = 0; s < 8; ++s) {
            if (s < 6) st(s + 2);
            LAS const char* b0 = lds + (s >> 2) * KVP1 + wid * 4096 + (16 * (s & 3) + 8 * h + q) * 64 + 32 * blk + 8 * pq;
            const bf16x8 wf = cat8(lds_tr(b0), lds_tr(b0 + 256));
            if (isv) ak = mfma32(xf[s], wf, ak); else ak = mfma32(wf, xf[s], ak);
        }
        asm volatile("s_waitcnt lgkmcnt(0)" ::: "memory");
#pragma unroll
        for (int i = 0; i < 4; ++i) *(LAS f32x4*)(lds + wid * 4096 + i * 1024 + lane * 16) = (f32x4){ak[4 * i], ak[4 * i + 1], ak[4 * i + 2], ak[4 * i + 3]};
        asm volatile("s_waitcnt lgkmcnt(0)\n\ts_barrier" ::: "memory");
        if (wid < 2) {
            f32x4 s0 = {0.f, 0.f, 0.f, 0.f}, s1 = {0.f, 0.f, 0.f, 0.f};
#pragma unroll
            for (int w = 0; w < 8; ++w) { s0 += *(LAS const f32x4*)(lds + w * 4096 + (2 * wid) * 1024 + lane * 16); s1 += *(LAS const f32x4*)(lds + w * 4096 + (2 * wid + 1) * 1024 + lane * 16); }
            u32x4 o; o.x = cvtpk(s0[0], s0[1]); o.y = cvtpk(s0[2], s0[3]); o.z = cvtpk(s1[0], s1[1]); o.w = cvtpk(s1[2], s1[3]);
            *(u32x4*)(p.ws + (isv ? WS_VA : WS_KA) + (size_t)(tb * 2 + wid) * 1024 + lane * 16) = o;
        }
    }
};

__global__ __launch_bounds__(512, 2) void k_proj(P1 p) {
    extern __shared__ __attribute__((aligned(16))) char smem[];
    LAS char* lds = (LAS char*)smem;
    const int tid = threadIdx.x, wid = __builtin_amdgcn_readfirstlane(tid >> 6), lane = tid & 63;
    const int bid = blockIdx.x, xcd = bid & 7, idx = bid >> 3;
    STAMP(p.ws, 0, 0);
    CODE_PF("_Z6k_proj2P1", "_Z6k_attn2P2-_Z6k_proj2P1", wid, lds + 131072 + wid * 256);
    if (bid == 0 && tid < 128) ((unsigned*)(p.ws + WS_FLAG))[tid * 16] = 0u;
    const int wr = wid >> 2, wc = wid & 3;
    const int pm = idx & 7, pn = 2 * xcd + ((idx >> 3) & 1), kind = idx >> 4;
    const int gw = bid * 8 + wid;
    const char* warm = gw < 1365 ? (const char*)p.xA + (size_t)gw * 3072 : (const char*)p.wqA + (size_t)(gw - 1365 < 681 ? gw - 1365 : 681) * 3072;
    f32x16 acc[4][2];
    auto store_f = [&](char* base, float sc) {
#pragma unroll
        for (int m = 0; m < 4; ++m)
#pragma unroll
            for (int n = 0; n < 2; ++n) { const int tb = pm * 8 + wr * 4 + m, fb = pn * 8 + wc * 2 + n, hd = fb >> 2, db = fb & 3;
                char* dst = base + (size_t)((hd * NTB + tb) * 8 + 2 * db) * 1024 + lane * 16;
                st16(dst, pack8(acc[m][n], 0, sc)); st16(dst + 1024, pack8(acc[m][n], 8, sc)); }
    };
    if (kind == 0) {
        KvUnit kv; const int tbk = xcd * 8 + pm, isv = (idx >> 3) & 1;
        big_stream<false>(lds, (const char*)p.xB + (size_t)pm * 256 * 2048, 2048, (const char*)p.wqB + pn * 512, nullptr, acc,
                          [&](int part) { kv.issue(part, lds, p, tbk, isv, wid, lane); }, warm, (const char*)(isv ? p.wvA : p.wkA) + wid * 8192, lane * 128, wid, lane);
        SBAR();
        kv.finish(lds, p, tbk, isv, wid, lane, [&](int g) { const int m = g >> 1, n = g & 1, tb = pm * 8 + wr * 4 + m, fb = pn * 8 + wc * 2 + n, hd = fb >> 2, db = fb & 3;
            char* dst = p.ws + WS_QB + (size_t)((hd * NTB + tb) * 8 + 2 * db) * 1024 + lane * 16;
            st16(dst, pack8(acc[m][n], 0, CB)); st16(dst + 1024, pack8(acc[m][n], 8, CB)); });
    } else {
        unsigned long long pc; int oa, oo;
        asm volatile("s_getpc_b64 %0\n.Lpc_anchor_%=:\n\ts_mov_b32 %1, _Z6k_attn2P2-.Lpc_anchor_%=\n\ts_mov_b32 %2, _Z5k_out2P3-.Lpc_anchor_%=" : "=s"(pc), "=s"(oa), "=s"(oo));
        const unsigned long long ca = pc + (long long)oa, co = pc + (long long)oo;
        const long lim = (long)(co - ca) + 4096 - 128;
        const char* w2 = (const char*)ca; unsigned w2off = (wid & 3) * 8192 + lane * 128; if (lim < 0) w2off = 0; else if ((long)w2off > lim) w2off = (unsigned)lim;
        big_stream<true>(lds, (const char*)p.cKV + (size_t)pm * 256 * 1024, 1024, (const char*)p.wukB + pn * 512, (const char*)p.wuvB + pn * 512, acc,
                         [&](int) { store_f(p.ws + WS_KB, 1.f); }, warm, w2, w2off, wid, lane);
#pragma unroll
        for (int m = 0; m < 4; ++m)
#pragma unroll
            for (int n = 0; n < 2; ++n) { const int tb = pm * 8 + wr * 4 + m, fb = pn * 8 + wc * 2 + n, hd = fb >> 2, db = fb & 3;
                char* dst = p.ws + WS_VB + (size_t)((hd * NTB + tb) * 8 + db) * 1024 + lane * 16;
                st16(dst, pack8(acc[m][n], 0, 1.f)); st16(dst + 4096, pack8(acc[m][n], 8, 1.f)); }
    }
    STAMP(p.ws, 0, 1); STAMP(p.ws, 0, 2); STAMP(p.ws, 0, 3);
}

struct P2 { const u16 *xA, *wqA, *woA, *woB; char* ws; };

DI void exp16(const f32x16& s, unsigned (&pk)[8], float& t0, float& t1, unsigned& orv) {
#pragma unroll
    for (int i = 0; i < 8; ++i) {
        const float a = __builtin_amdgcn_exp2f(s[2 * i]), b = __builtin_amdgcn_exp2f(s[2 * i + 1]);
        t0 += a; t1 += b; pk[i] = cvtpk(a, b); orv |= pk[i];
    }
}
DI float max16(const f32x16& s) {
    float m = fmaxf(s[0], s[1]);
#pragma unroll
    for (int i = 2; i < 16; ++i) m = fmaxf(m, s[i]);
    return m;
}
DI void diag_mask(f32x16& s, int r, int h) {
#pragma unroll
    for (int i = 0; i < 16; ++i) { const int kr = (i & 3) + 8 * (i >> 2) + 4 * h; if (kr > r) s[i] = -__builtin_inff(); }
}
DI bf16x8 pfrag(const unsigned (&pk)[8], int b) { u32x4 w = {pk[b], pk[b + 1], pk[b + 2], pk[b + 3]}; return __builtin_bit_cast(bf16x8, w); }

DI void attnB(LAS char* lds, char* ws, int hd, int xb, int wid, int lane) {
    const int r = lane & 31, h = lane >> 5;
    const int tbq = 4 * xb + wid, NT = 2 * (xb + 1);
    const char* kbase = ws + WS_KB + (size_t)hd * NTB * 8192 + wid * 4096;
    const char* vbase = ws + WS_VB + (size_t)hd * NTB * 8192 + wid * 4096;
    const unsigned lds0 = (unsigned)(size_t)lds + wid * 4096, lane16 = lane * 16;
    auto piece = [&](const char* src, unsigned m0v, int i) { if (i == 0) glds16o<0>(src, lane16, m0v); else if (i == 1) glds16o<1024>(src, lane16, m0v); else if (i == 2) glds16o<2048>(src, lane16, m0v); else glds16o<3072>(src, lane16, m0v); };
    glds16x4(kbase, lane * 16, lds + wid * 4096); glds16x4(vbase, lane * 16, lds + 32768 + wid * 4096); glds16x4(kbase + 16384, lane * 16, lds + 16384 + wid * 4096);
    bf16x8 qf[8];
#pragma unroll
    for (int s = 0; s < 8; ++s) qf[s] = *(const bf16x8*)(ws + WS_QB + (size_t)((hd * NTB + tbq) * 8 + s) * 1024 + lane * 16);
    f32x16 o[4], negm, S0, S1; float l = 0.f, t0, t1;
    bf16x8 fr[8]; unsigned pk[8], pn[4];
#pragma unroll
    for (int d = 0; d < 4; ++d) o[d] = f32x16{};
    LAS const char* lbase = lds + lane * 16;
    auto kaddr = [&](int j) { return lbase + ((j >> 1) & 1) * 16384 + (j & 1) * 8192; };
    auto vaddr = [&](int j) { return lbase + 32768 + ((j >> 1) & 1) * 16384 + (j & 1) * 8192; };
    auto slotA = [&](auto ST, f32x16& Sc, f32x16& Sn, int j, const char* src, unsigned m0v) {
        LAS const char* va = vaddr(j);
        float ea = 0.f, eb = 0.f;
#pragma unroll
        for (int i = 0; i < 4; ++i) pk[i] = pn[i];
#pragma unroll
        for (int i = 0; i < 8; ++i) {
            Sn = mfma32(fr[i], qf[i], i ? Sn : negm);
            fr[i] = lds_b128(va + i * 1024);
            if (!(i & 1)) { ea = __builtin_amdgcn_exp2f(Sc[8 + i]); eb = __builtin_amdgcn_exp2f(Sc[9 + i]); PIN(ea); PIN(eb); if (ST.value) piece(src, m0v, i >> 1); }
            else { pk[4 + (i >> 1)] = cvtpk(ea, eb); t0 += ea; t1 += eb; PIN(t0); PIN(t1); }
            SBAR();
        }
        float ts = t0 + t1;
        if (__builtin_expect(__any(!(ts < 1.f)), 0)) {
            const float delta = fmaxf(swap32_max(max16(Sc)) + SM_BIAS, 0.f), alpha = __builtin_amdgcn_exp2f(-delta);
            l *= alpha;
#pragma unroll
            for (int d = 0; d < 4; ++d) o[d] *= alpha;
            negm -= delta; Sn -= delta; t0 = 0.f; t1 = 0.f;
#pragma unroll
            for (int i = 0; i < 8; ++i) { const float a = __builtin_amdgcn_exp2f(Sc[2 * i] - delta), b = __builtin_amdgcn_exp2f(Sc[2 * i + 1] - delta); pk[i] = cvtpk(a, b); t0 += a; t1 += b; }
            ts = t0 + t1;
        }
        l += ts;
        if (j + 1 == tbq) diag_mask(Sn, r, h);
    };
    auto slotB = [&](auto ST, f32x16& Sn, int j, const char* src, unsigned m0v) {
        LAS const char* ka = kaddr(j + 2);
        float ea = 0.f, eb = 0.f;
        t0 = 0.f; t1 = 0.f;
#pragma unroll
        for (int i = 0; i < 8; ++i) {
            o[i & 3] = mfma32(fr[i], pfrag(pk, 4 * (i >> 2)), o[i & 3]);
            fr[i] = lds_b128(ka + i * 1024);
            if (!(i & 1)) { if (ST.value) piece(src, m0v, i >> 1); ea = __builtin_amdgcn_exp2f(Sn[i]); eb = __builtin_amdgcn_exp2f(Sn[i + 1]); PIN(ea); PIN(eb); }
            else { pn[i >> 1] = cvtpk(ea, eb); t0 += ea; t1 += eb; PIN(pn[i >> 1]); PIN(t0); PIN(t1); }
            SBAR();
        }
    };
    using TT = std::integral_constant<bool, true>; using FF = std::integral_constant<bool, false>;
    WAIT_BAR(0);
    {
        S0 = f32x16{};
#pragma unroll
        for (int s = 0; s < 8; ++s) fr[s] = lds_b128(lbase + s * 1024);
#pragma unroll
        for (int s = 0; s < 8; ++s) S0 = mfma32(fr[s], qf[s], S0);
#pragma unroll
        for (int s = 0; s < 8; ++s) fr[s] = lds_b128(lbase + 8192 + s * 1024);
        if (tbq == 0) diag_mask(S0, r, h);
        const float nm = -(swap32_max(max16(S0)) + SM_BIAS);
#pragma unroll
        for (int i = 0; i < 16; ++i) negm[i] = nm;
        S0 += nm;
        t0 = 0.f; t1 = 0.f;
#pragma unroll
        for (int i = 0; i < 4; ++i) { const float a = __builtin_amdgcn_exp2f(S0[2 * i]), b = __builtin_amdgcn_exp2f(S0[2 * i + 1]); pn[i] = cvtpk(a, b); t0 += a; t1 += b; }
    }
    for (int j = 0;; j += 2) {
        const int t = j >> 1;
        WAIT_BAR(0);
        if ((t & 3) != 3) __builtin_amdgcn_s_setprio(2); else __builtin_amdgcn_s_setprio(0);
        const int tk = t + 2 < NT ? t + 2 : NT - 1, tv = t + 1 < NT ? t + 1 : NT - 1;
        slotA(TT{}, S0, S1, j, kbase + (size_t)tk * 16384, lds0 + (t & 1) * 16384);  slotB(TT{}, S1, j, vbase + (size_t)tv * 16384, lds0 + 32768 + ((t + 1) & 1) * 16384);
        if (j == tbq) break;
        slotA(FF{}, S1, S0, j + 1, nullptr, 0u); slotB(FF{}, S0, j + 1, nullptr, 0u);
        if (j + 1 == tbq) break;
    }
    if (wid < 2) WAIT_BAR(0);
    asm volatile("s_waitcnt vmcnt(0)" ::: "memory");
    const float inv = 1.f / swap32_add(l);
    char* dst = ws + WS_CTX + (size_t)(tbq * 256 + hd * 8) * 1024 + lane * 16;
#pragma unroll
    for (int d = 0; d < 4; ++d) { st16(dst + (2 * d) * 1024, pack8(o[d], 0, inv)); st16(dst + (2 * d + 1) * 1024, pack8(o[d], 8, inv)); }
}

DI void projA(LAS char* lds, const P2& p, int hg, int tg, bf16x8 (&qf)[2][2], int wid, int lane) {
    constexpr int STG = 24576, NST = 16;
    const int r = lane & 31, h = lane >> 5;
    const char* xsrc = (const char*)p.xA + (size_t)tg * 64 * 2048;
    unsigned voX[2], voW[4];
#pragma unroll
    for (int i = 0; i < 2; ++i) { const int row = (wid * 2 + i) * 8 + (lane >> 3), ch = (lane & 7) ^ ((row >> 1) & 7); voX[i] = (unsigned)row * 2048u + ch * 16; }
#pragma unroll
    for (int i = 0; i < 4; ++i) { const int k = ((wid & 1) * 4 + i) * 8 + (lane >> 3), slot = lane & 7;
        voW[i] = (unsigned)k * 2048u + (128 * hg + 64 * (wid >> 1) + 32 * ((slot >> 2) ^ ((k >> 1) & 1)) + 8 * (slot & 3)) * 2; }
    auto stage1 = [&](int slot, int t, int i) {
        LAS char* base = lds + slot * STG;
        if (i < 2) glds16(xsrc + (size_t)t * 128, voX[i], base + (wid * 2 + i) * 1024);
        else glds16((const char*)p.wqA + (size_t)t * 64 * 2048, voW[i - 2], base + 8192 + (wid >> 1) * 8192 + ((wid & 1) * 4 + i - 2) * 1024);
    };
    int aoff[4];
#pragma unroll
    for (int s = 0; s < 4; ++s) aoff[s] = r * 128 + (((2 * s + h) ^ ((r >> 1) & 7)) << 4);
    const int blk = (lane >> 4) & 1, q = (lane & 15) >> 2, pq = lane & 3;
    const int boff = 8192 + (wid >> 1) * 8192 + (8 * h + q) * 128 + 64 * ((wid & 1) ^ (q >> 1)) + 32 * blk + 8 * pq;
    f32x16 acc[2]; acc[0] = f32x16{}; acc[1] = f32x16{};
    bf16x8 wf[4], a0[4], a1[4];
    auto rd = [&](int slot, int s) { LAS const char* sa = lds + slot * STG;
        wf[s] = cat8(lds_tr(sa + boff + s * 2048), lds_tr(sa + boff + s * 2048 + 512)); a0[s] = lds_b128(sa + aoff[s]); a1[s] = lds_b128(sa + aoff[s] + 4096); };
#pragma unroll
    for (int t = 0; t < 3; ++t)
#pragma unroll
        for (int i = 0; i < 6; ++i) stage1(t, t, i);
    WAIT_BAR(12);
    rd(0, 0); rd(0, 1);
    int slot = 0;
    for (int t = 0; t < NST; ++t) {
        int sn = slot + 1; if (sn == 3) sn = 0;
        rd(slot, 2); acc[0] = mfma32(wf[0], a0[0], acc[0]); acc[1] = mfma32(wf[0], a1[0], acc[1]); SBAR();
        rd(slot, 3); acc[0] = mfma32(wf[1], a0[1], acc[0]); acc[1] = mfma32(wf[1], a1[1], acc[1]); SBAR();
        if (t + 2 < NST) { WAIT_BAR(6); } else { WAIT_BAR(0); }
        rd(sn, 0); acc[0] = mfma32(wf[2], a0[2], acc[0]); acc[1] = mfma32(wf[2], a1[2], acc[1]); SBAR();
        if (t + 3 < NST) { stage1(slot, t + 3, 0); stage1(slot, t + 3, 1); stage1(slot, t + 3, 2); }
        rd(sn, 1); acc[0] = mfma32(wf[3], a0[3], acc[0]); acc[1] = mfma32(wf[3], a1[3], acc[1]); SBAR();
        if (t + 3 < NST) { stage1(slot, t + 3, 3); stage1(slot, t + 3, 4); stage1(slot, t + 3, 5); }
        slot = sn;
    }
#pragma unroll
    for (int u = 0; u < 2; ++u)
#pragma unroll
        for (int s = 0; s < 2; ++s) qf[u][s] = __builtin_bit_cast(bf16x8, pack8(acc[u], 8 * s, CA));
    WAIT_BAR(0);
}
DI void attnA(LAS char* lds, const P2& p, int hg, int tg, int wid, int lane) {
    char* ws = p.ws;
    const int hd = 4 * hg + wid, tb0 = 2 * tg;
    bf16x8 qf[2][2];
    __builtin_amdgcn_s_setprio(3);
    projA(lds, p, hg, tg, qf, wid, lane);
    STAMP(ws, 1, 1);
    __builtin_amdgcn_s_setprio(1);
    STAMP(ws, 1, 2);
    const char* kvsrc = ws + ((wid & 2) ? WS_VA : WS_KA) + (wid & 1) * 4096;
    auto stage = [&](int slot, int t) { glds16x4(kvsrc + (size_t)t * 8192, lane * 16, lds + slot * 16384 + wid * 4096); };
    stage(0, 0); stage(1, 1);
    f32x16 o[2], negm[2], Sa[2], Sb[2]; float l[2] = {0.f, 0.f};
    o[0] = f32x16{}; o[1] = f32x16{};
    bf16x8 kf[2], vf[2]; unsigned pp[2][8];
    LAS const char* lbase = lds + lane * 16;
    auto step = [&](f32x16 (&Sc)[2], f32x16 (&Sn)[2], LAS const char* ka, LAS const char* va) {
        unsigned pn[2][8]; float t[2][2] = {{0.f, 0.f}, {0.f, 0.f}}, e[2][4];
#pragma unroll
        for (int i = 0; i < 9; ++i) {
            if (i < 8) {
                const int u = i & 1, s = (i >> 1) & 1;
                if (i < 4) Sn[u] = mfma32(kf[s], qf[u][s], s ? Sn[u] : negm[u]);
                else o[u] = mfma32(vf[s], pfrag(pp[u], 4 * s), o[u]);
                if (i == 3) { kf[0] = lds_b128(ka); kf[1] = lds_b128(ka + 1024); }
                if (i == 7) { vf[0] = lds_b128(va); vf[1] = lds_b128(va + 1024); }
#pragma unroll
                for (int k = 0; k < 4; ++k) { e[i & 1][k] = __builtin_amdgcn_exp2f(Sc[i >> 2][(i & 3) * 4 + k]); PIN(e[i & 1][k]); }
            }
            if (i > 0) {
                const int g = i - 1, uu = g >> 2, w = (g & 3) * 2; const float* x = e[g & 1];
                pn[uu][w] = cvtpk(x[0], x[1]); pn[uu][w + 1] = cvtpk(x[2], x[3]);
                t[uu][0] += x[0]; t[uu][1] += x[1]; t[uu][0] += x[2]; t[uu][1] += x[3]; PIN(t[uu][0]); PIN(t[uu][1]); PIN(pn[uu][w]); PIN(pn[uu][w + 1]);
            }
            if (i < 8) SBAR();
        }
        float ts[2] = {t[0][0] + t[0][1], t[1][0] + t[1][1]};
        if (__builtin_expect(__any(!(ts[0] + ts[1] < 1.f)), 0)) {
#pragma unroll
            for (int u = 0; u < 2; ++u) {
                const float dl = fmaxf(swap32_max(max16(Sc[u])) + SM_BIAS, 0.f), al = __builtin_amdgcn_exp2f(-dl);
                l[u] *= al; o[u] *= al; negm[u] -= dl; Sn[u] -= dl; float a0 = 0.f, a1 = 0.f;
#pragma unroll
                for (int i = 0; i < 8; ++i) { const float a = __builtin_amdgcn_exp2f(Sc[u][2 * i] - dl), b = __builtin_amdgcn_exp2f(Sc[u][2 * i + 1] - dl); pn[u][i] = cvtpk(a, b); a0 += a; a1 += b; }
                ts[u] = a0 + a1;
            }
        }
#pragma unroll
        for (int u = 0; u < 2; ++u) { l[u] += ts[u];
#pragma unroll
            for (int i = 0; i < 8; ++i) pp[u][i] = pn[u][i]; }
    };
    WAIT_BAR(4);
    {
        kf[0] = lds_b128(lbase); kf[1] = lds_b128(lbase + 1024);
#pragma unroll
        for (int u = 0; u < 2; ++u) {
            Sa[u] = mfma32(kf[0], qf[u][0], f32x16{}); Sa[u] = mfma32(kf[1], qf[u][1], Sa[u]);
            const float nm = -(swap32_max(max16(Sa[u])) + SM_BIAS);
#pragma unroll
            for (int i = 0; i < 16; ++i) negm[u][i] = nm;
            Sa[u] += nm;
#pragma unroll
            for (int i = 0; i < 8; ++i) pp[u][i] = 0u;
        }
        kf[0] = lds_b128(lbase + 2048); kf[1] = lds_b128(lbase + 2048 + 1024);
        vf[0] = kf[0]; vf[1] = kf[1];
    }
    int slot = 0;
    for (int t = 0; t < 16; ++t) {
        WAIT_BAR(0);
        int sn = slot + 1; if (sn == 3) sn = 0; int sp = sn + 1; if (sp == 3) sp = 0;
        if (t + 2 < 16) stage(sp, t + 2);
        if (t == 2) { const int gw = (hg * 32 + tg) * 4 + wid; l2pf((const char*)p.woB + (size_t)gw * 8192, lane * 128, lds + 49152 + wid * 256);
                      l2pf((const char*)p.woA + (size_t)gw * 2048, (lane & 15) * 128, lds + 49152 + wid * 256); }
        LAS const char* cur = lbase + slot * 16384; LAS const char* nxt = lbase + sn * 16384;
        step(Sa, Sb, cur + 2 * 2048, cur + 8192);
        step(Sb, Sa, cur + 3 * 2048, cur + 8192 + 2048);
        step(Sa, Sb, nxt, cur + 8192 + 2 * 2048);
        step(Sb, Sa, nxt + 2048, cur + 8192 + 3 * 2048);
        slot = sn;
    }
#pragma unroll
    for (int s = 0; s < 2; ++s)
#pragma unroll
        for (int u = 0; u < 2; ++u) o[u] = mfma32(vf[s], pfrag(pp[u], 4 * s), o[u]);
#pragma unroll
    for (int u = 0; u < 2; ++u) {
        const float inv = 1.f / swap32_add(l[u]);
        char* dst = ws + WS_OA + (size_t)((tb0 + u) * 64 + hd * 2) * 1024 + lane * 16;
        st16(dst, pack8(o[u], 0, inv)); st16(dst + 1024, pack8(o[u], 8, inv));
    }
}

__global__ __launch_bounds__(256, 2) void k_attn(P2 p) {
    extern __shared__ __attribute__((aligned(16))) char smem[];
    LAS char* lds = (LAS char*)smem;
    const int tid = threadIdx.x, wid = __builtin_amdgcn_readfirstlane(tid >> 6), lane = tid & 63;
    const int bid = blockIdx.x;
    const int xcd = bid & 7, idx = bid >> 3,
        kind = (idx ^ (idx >> 5)) & 1,
        it = (idx >> 1) & 15 | (idx >> 5) << 4;
    STAMP(p.ws, 1, 0);
    CODE_PF("_Z6k_attn2P2", "_Z5k_out2P3-_Z6k_attn2P2", wid, lds + 73728 + wid * 256);
    if (kind == 0) { attnA(lds, p, xcd, it, wid, lane); }
    else {
        const int hd = xcd + 8 * (it >> 3), x = it & 7;
        attnB(lds, p.ws, hd, 15 - x, wid, lane);
        WAIT_BAR(0);
        STAMP(p.ws, 1, 1);
        attnB(lds, p.ws, hd, x, wid, lane); STAMP(p.ws, 1, 2); }
    STAMP(p.ws, 1, 3);
}

struct P3 { const u16 *woA, *woB; char* ws; u16* out; };

constexpr int NT1 = 24;
constexpr int PUBD = 4, POLLD = 5;
DI void out_store(LAS char*, u16* out, int tb0, int col0, const f32x16 (&acc)[2][2], int wid, int lane) {
    const int wm = wid >> 1, wn = wid & 1, r = lane & 31, h = lane >> 5;
#pragma unroll
    for (int m = 0; m < 2; ++m) {
        char* dst = (char*)out + (size_t)((tb0 + 2 * wm + m) * 32 + r) * 4096 + (col0 + 64 * wn + 8 * h) * 2;
#pragma unroll
        for (int c = 0; c < 2; ++c) { *(u32x4*)(dst + c * 64) = pack8(acc[m][c], 0, 1.f); *(u32x4*)(dst + c * 64 + 32) = pack8(acc[m][c], 8, 1.f); }
    }
}

__global__ __launch_bounds__(512, 2) void k_out(P3 p) {
    extern __shared__ __attribute__((aligned(16))) char smem[];
    LAS char* lds = (LAS char*)smem;
    constexpr int STG = 32768, nt = 40;
    const int tid = threadIdx.x, wid = __builtin_amdgcn_readfirstlane(tid >> 6), lane = tid & 63;
    const int bid = blockIdx.x, xg = bid & 7, role = (xg >> 2) ^ 1, ti = bid >> 3;
    const int pm = 8 * ((xg >> 1) & 1) + (ti >> 2), pn = 4 * (xg & 1) + (ti & 3), tb0 = pm * 4, tau = pm * 8 + pn;
    STAMP(p.ws, 2, 0);
    if (wid >= 4) {
        CODE_PF("_Z5k_out2P3", "6144", 0, lds + 131072 + (wid - 4) * 256);
        const int lw = wid - 4;
        const char* a0 = p.ws + WS_CTX + (size_t)((tb0 + lw) * 256 + (role ? 4 * (64 - NT1) : 0)) * 1024;
        const char* w0 = (const char*)p.woB + pn * 256 + (size_t)(role ? 64 - NT1 : 0) * 64 * 2048;
        const char* a1 = p.ws + WS_OA + (size_t)((tb0 + lw) * 64) * 1024 - (size_t)NT1 * 4096;
        const char* w1 = (const char*)p.woA + pn * 256 - (size_t)NT1 * 64 * 2048;
        const int tsw = role ? NT1 : nt;
        unsigned voW[4];
#pragma unroll
        for (int i = 0; i < 4; ++i) { const int pc = 4 * lw + i, g = pc >> 3, k = (pc & 7) * 8 + (lane >> 3), slot = lane & 7;
            const int col = 64 * g + 32 * ((slot >> 2) ^ ((k >> 1) & 1)) + 8 * (slot & 3); voW[i] = (unsigned)k * 2048u + col * 2; }
        auto stage = [&](int t) {
            LAS char* base = lds + (t & 3) * STG;
            const char* as = (t < tsw ? a0 : a1) + (size_t)t * 4096; const char* wsd = (t < tsw ? w0 : w1) + (size_t)t * (64 * 2048);
            glds16x4(as, lane * 16, base + lw * 4096);
#pragma unroll
            for (int i = 0; i < 4; ++i) glds16(wsd, voW[i], base + 16384 + (4 * lw + i) * 1024);
        };
#pragma unroll
        for (int t = 0; t < 4; ++t) stage(t);
        WAIT_BAR(24);
        for (int t = 0; t < nt; ++t) {
            if (t + 3 < nt) { WAIT_BAR(16); } else if (t + 2 < nt) { WAIT_BAR(8); } else { WAIT_BAR(0); }
            if (t + 4 < nt) stage(t + 4);
        }
    } else {
        const int wm = wid >> 1, wn = wid & 1, h = lane >> 5;
        const int blk = (lane >> 4) & 1, q = (lane & 15) >> 2, pq = lane & 3, pp = ((pq & 1) << 1) | (pq >> 1);
        GAS unsigned* flag = (GAS unsigned*)(p.ws + WS_FLAG) + tau * 16;
        const __amdgpu_buffer_rsrc_t rs = __builtin_amdgcn_make_buffer_rsrc(p.ws + WS_PART + (size_t)tau * 65536, 0, 65536, 0x00020000);
        int boff[2];
#pragma unroll
        for (int c = 0; c < 2; ++c) boff[c] = 16384 + wn * 8192 + (4 * h + q) * 128 + 64 * (c ^ (q >> 1)) + 32 * blk + 8 * pp;
        const int aoff = (2 * wm * 4) * 1024 + lane * 16;
        f32x16 acc[2][2];
#pragma unroll
        for (int m = 0; m < 2; ++m) { acc[m][0] = f32x16{}; acc[m][1] = f32x16{}; }
        bf16x8 af[4][2], wf[4][2];
        auto rd = [&](int slot, int s, int i) {
            LAS const char* sa = lds + slot * STG;
            if (i & 1) af[s][i >> 1] = lds_b128(sa + aoff + ((i >> 1) * 4 + s) * 1024);
            else wf[s][i >> 1] = cat8(lds_tr(sa + boff[i >> 1] + s * 2048), lds_tr(sa + boff[i >> 1] + s * 2048 + 1024));
        };
        auto kstep = [&](int s, int nslot) {
#pragma unroll
            for (int i = 0; i < 4; ++i) {
                const int m = i >> 1, c = i & 1;
                acc[m][c] = mfma32(wf[s][c], af[s][m], acc[m][c]);
                rd(nslot, (s + 2) & 3, i);
                SBAR();
            }
        };
        auto tiles = [&](int t0, int t1) {
#pragma unroll 1
            for (int t = t0; t < t1; ++t) {
                kstep(0, t & 3); kstep(1, t & 3);
                asm volatile("s_waitcnt lgkmcnt(0)\n\ts_barrier" ::: "memory");
                kstep(2, (t + 1) & 3); kstep(3, (t + 1) & 3);
            }
        };
        WAIT_BAR(0);
#pragma unroll
        for (int i = 0; i < 4; ++i) { rd(0, 0, i); rd(0, 1, i); }
        if (role == 1) {
            tiles(0, NT1);
#pragma unroll
            for (int m = 0; m < 2; ++m)
#pragma unroll
                for (int c = 0; c < 2; ++c) {
#pragma unroll
                    for (int i = 0; i < 4; ++i) { u32x4 v;
#pragma unroll
                        for (int j = 0; j < 4; ++j) v[j] = __float_as_uint(acc[m][c][4 * i + j]);
                        __builtin_amdgcn_raw_buffer_store_b128(v, rs, wid * 16384 + ((m * 2 + c) * 4 + i) * 1024 + lane * 16, 0, 16); }
                    acc[m][c] = f32x16{}; }
            tiles(NT1, NT1 + PUBD);
            asm volatile("s_waitcnt vmcnt(0)" ::: "memory");
            tiles(NT1 + PUBD, NT1 + PUBD + 1);
            if (tid == 0) __hip_atomic_store(flag, 1u, __ATOMIC_RELAXED, __HIP_MEMORY_SCOPE_AGENT);
            STAMP(p.ws, 2, 1);
            tiles(NT1 + PUBD + 1, nt);
            STAMP(p.ws, 2, 2);
            out_store(lds + 32768 + wid * 8192, p.out, tb0, pn * 128, acc, wid, lane);
        } else {
            tiles(0, nt - POLLD);
            while ((unsigned)__builtin_amdgcn_readfirstlane(__hip_atomic_load(flag, __ATOMIC_RELAXED, __HIP_MEMORY_SCOPE_AGENT)) != 1u) __builtin_amdgcn_s_sleep(2);
            __builtin_amdgcn_fence(__ATOMIC_ACQUIRE, "wavefront");
            u32x4 pv[16];
#pragma unroll
            for (int i = 0; i < 16; ++i) pv[i] = __builtin_amdgcn_raw_buffer_load_b128(rs, wid * 16384 + i * 1024 + lane * 16, 0, 16);
            STAMP(p.ws, 2, 1);
            tiles(nt - POLLD, nt);
#pragma unroll
            for (int i = 0; i < 16; ++i)
#pragma unroll
                for (int j = 0; j < 4; ++j) acc[i >> 3][(i >> 2) & 1][4 * (i & 3) + j] += __uint_as_float(pv[i][j]);
            STAMP(p.ws, 2, 2);
            out_store(lds + 32768 + wid * 8192, p.out, tb0, 1024 + pn * 128, acc, wid, lane);
        }
        STAMP(p.ws, 2, 3);
    }
}

#define BENCH_MARK(s)
extern "C" void kernel_launch(void* const* d_in, const int* in_sizes, int n_in, void* d_out, int out_size, void* d_ws, size_t ws_size,
                              hipStream_t stream) {
    (void)in_sizes; (void)n_in; (void)out_size; (void)ws_size;
    (void)hipFuncSetAttribute((const void*)k_proj, hipFuncAttributeMaxDynamicSharedMemorySize, 133120);
    (void)hipFuncSetAttribute((const void*)k_attn, hipFuncAttributeMaxDynamicSharedMemorySize, 74752);
    (void)hipFuncSetAttribute((const void*)k_out, hipFuncAttributeMaxDynamicSharedMemorySize, 132096);
    P1 p1; p1.wqA = (const u16*)d_in[1]; p1.xA = (const u16*)d_in[0]; p1.wkA = (const u16*)d_in[2]; p1.wvA = (const u16*)d_in[3];
    p1.xB = (const u16*)d_in[5]; p1.cKV = (const u16*)d_in[6]; p1.wqB = (const u16*)d_in[7]; p1.wukB = (const u16*)d_in[8]; p1.wuvB = (const u16*)d_in[9];
    p1.ws = (char*)d_ws;
    BENCH_MARK(stream);
    k_proj<<<256, 512, 133120, stream>>>(p1);
    BENCH_MARK(stream);
    P2 p2; p2.woA = (const u16*)d_in[4]; p2.woB = (const u16*)d_in[10]; p2.xA = (const u16*)d_in[0]; p2.wqA = (const u16*)d_in[1]; p2.ws = (char*)d_ws;
    k_attn<<<512, 256, 74752, stream>>>(p2);
    BENCH_MARK(stream);
    P3 p3; p3.woA = (const u16*)d_in[4]; p3.woB = (const u16*)d_in[10]; p3.ws = (char*)d_ws; p3.out = (u16*)d_out;
    k_out<<<256, 512, 132096, stream>>>(p3);
    BENCH_MARK(stream);
}
```

```cpp
#include <hip/hip_runtime.h>
#include <hip/hip_bf16.h>
#include <type_traits>

typedef unsigned short u16;
typedef short bf16x8 __attribute__((ext_vector_type(8)));
typedef short bf16x4 __attribute__((ext_vector_type(4)));
typedef float f32x16 __attribute__((ext_vector_type(16)));
typedef float f32x4 __attribute__((ext_vector_type(4)));
typedef float f32x2 __attribute__((ext_vector_type(2)));
typedef __bf16 bf16x2_t __attribute__((ext_vector_type(2)));
typedef unsigned u32x4 __attribute__((ext_vector_type(4)));
#define LAS __attribute__((address_space(3)))
#define GAS __attribute__((address_space(1)))
#define DI __device__ __forceinline__

DI f32x16 mfma32(bf16x8 a, bf16x8 b, f32x16 c) { return __builtin_amdgcn_mfma_f32_32x32x16_bf16(a, b, c, 0, 0, 0); }
DI unsigned cvtpk(float lo, float hi) { f32x2 v = {lo, hi}; bf16x2_t b = __builtin_convertvector(v, bf16x2_t); return __builtin_bit_cast(unsigned, b); }
DI void glds16(const void* sbase, unsigned voff, LAS void* l) {
    unsigned sv; asm volatile("s_mov_b32 %0, m0\n\ts_mov_b32 m0, %3\n\ts_nop 0\n\tglobal_load_lds_dwordx4 %1, %2\n\ts_mov_b32 m0, %0"
                              : "=&s"(sv) : "v"(voff), "s"(sbase), "s"((unsigned)(size_t)l) : "memory"); }
template <int OFF> DI void glds16o(const void* sbase, unsigned voff, unsigned m0v) {
    asm volatile("s_mov_b32 m0, %2\n\ts_nop 0\n\tglobal_load_lds_dwordx4 %0, %1 offset:%3" :: "v"(voff), "s"(sbase), "s"(m0v), "i"(OFF) : "memory"); }
DI void l2pf(const void* sbase, unsigned voff, LAS void* scratch) {
    unsigned sv; asm volatile("s_mov_b32 %0, m0\n\ts_mov_b32 m0, %3\n\ts_nop 0\n\tglobal_load_lds_dword %1, %2\n\ts_mov_b32 m0, %0"
                              : "=&s"(sv) : "v"(voff), "s"(sbase), "s"((unsigned)(size_t)scratch) : "memory"); }
#define CODE_PF(SYM, LENEXPR, w, scratch) do { unsigned long long pc_; int o0_, o1_; \
    asm volatile("s_getpc_b64 %0\n.Lpcs_%=:\n\ts_mov_b32 %1, " SYM "-.Lpcs_%=\n\ts_mov_b32 %2, " LENEXPR : "=s"(pc_), "=s"(o0_), "=s"(o1_)); \
    int off_ = (w) * 8192 + lane * 128; if (off_ > o1_ - 128) off_ = o1_ - 128; if (off_ < 0) off_ = 0; \
    l2pf((const char*)(pc_ + (long long)o0_), (unsigned)off_, scratch); } while (0)
DI void glds16x4(const void* sbase, unsigned voff, LAS void* l) {
    unsigned sv; asm volatile("s_mov_b32 %0, m0\n\ts_mov_b32 m0, %3\n\ts_nop 0\n\tglobal_load_lds_dwordx4 %1, %2\n\tglobal_load_lds_dwordx4 %1, %2 offset:1024\n\t"
                              "global_load_lds_dwordx4 %1, %2 offset:2048\n\tglobal_load_lds_dwordx4 %1, %2 offset:3072\n\ts_mov_b32 m0, %0"
                              : "=&s"(sv) : "v"(voff), "s"(sbase), "s"((unsigned)(size_t)l) : "memory"); }
DI bf16x8 lds_b128(LAS const char* p) { return *(LAS const bf16x8*)p; }
DI bf16x4 lds_tr(LAS const char* p) { return __builtin_amdgcn_ds_read_tr16_b64_v4i16((LAS bf16x4*)p); }
DI bf16x8 cat8(bf16x4 a, bf16x4 b) { return (bf16x8){a[0], a[1], a[2], a[3], b[0], b[1], b[2], b[3]}; }
DI float swap32_max(float x) { auto rr = __builtin_amdgcn_permlane32_swap(__float_as_uint(x), __float_as_uint(x), false, false); return fmaxf(__uint_as_float(rr[0]), __uint_as_float(rr[1])); }
DI float swap32_add(float x) { auto rr = __builtin_amdgcn_permlane32_swap(__float_as_uint(x), __float_as_uint(x), false, false); return __uint_as_float(rr[0]) + __uint_as_float(rr[1]); }
#define WAIT_BAR(N) asm volatile("s_waitcnt vmcnt(" #N ") lgkmcnt(0)\n\ts_barrier" ::: "memory")
#define SBAR() __builtin_amdgcn_sched_barrier(0)
#define PIN(x) asm volatile("" : "+v"(x))

DI void st16(void* p, u32x4 v) { *(u32x4*)p = v; }
DI u32x4 pack8(const f32x16& a, int b, float sc) {
    u32x4 w; w.x = cvtpk(a[b + 0] * sc, a[b + 1] * sc); w.y = cvtpk(a[b + 2] * sc, a[b + 3] * sc);
    w.z = cvtpk(a[b + 4] * sc, a[b + 5] * sc); w.w = cvtpk(a[b + 6] * sc, a[b + 7] * sc); return w;
}

constexpr int SEQ = 2048, NTB = SEQ / 32;
constexpr float LOG2E = 1.4426950408889634f;
constexpr float CA = 0.17677669529663687f * LOG2E;
constexpr float CB = 0.08838834764831845f * LOG2E;
constexpr float SM_BIAS = 64.f;

constexpr size_t WS_QB = 0, WS_KB = WS_QB + (16u << 20), WS_VB = WS_KB + (16u << 20), WS_QA = WS_VB + (16u << 20),
                 WS_KA = WS_QA + (4u << 20), WS_VA = WS_KA + (128u << 10), WS_CTX = WS_VA + (128u << 10), WS_OA = WS_CTX + (16u << 20),
                 WS_PART = WS_OA + (4u << 20), WS_FLAG = WS_PART + (8u << 20), WS_END = WS_FLAG + 129 * 64;

#define STAMP(ws, kn, i)
struct P1 { const u16 *xB, *cKV, *wqB, *wukB, *wuvB, *xA, *wkA, *wvA, *wqA; char* ws; };

template <bool KV, class Mid>
DI void big_stream(LAS char* lds, const char* Ab, unsigned lda_b, const char* W0, const char* W1, f32x16 (&acc)[4][2], Mid mid, const char* warm, const char* warm2, unsigned w2off, int wid, int lane) {
    constexpr unsigned ldw_b = 8192;
    const int wr = wid >> 2, wc = wid & 3, r = lane & 31, h = lane >> 5;
    unsigned voA[4], voW[2];
#pragma unroll
    for (int i = 0; i < 4; ++i) { const int row = (wid * 4 + i) * 8 + (lane >> 3), ch = (lane & 7) ^ ((row >> 1) & 7); voA[i] = (unsigned)row * lda_b + ch * 16; }
#pragma unroll
    for (int i = 0; i < 2; ++i) {
        const int k = ((wid & 1) * 2 + i) * 8 + (lane >> 3), slot = lane & 7;
        const int col = 64 * (wid >> 1) + 32 * ((slot >> 2) ^ ((k >> 1) & 1)) + 8 * (slot & 3);
        voW[i] = (unsigned)k * ldw_b + col * 2;
    }
    int aoff[4], boff[2];
#pragma unroll
    for (int s = 0; s < 4; ++s) aoff[s] = (128 * wr + r) * 128 + (((2 * s + h) ^ ((r >> 1) & 7)) << 4);
    { const int blk = (lane >> 4) & 1, q = (lane & 15) >> 2, p = lane & 3;
#pragma unroll
      for (int n = 0; n < 2; ++n) boff[n] = 65536 + wc * 4096 + (8 * h + q) * 128 + 64 * (n ^ (q >> 1)) + 32 * blk + 8 * p; }
#pragma unroll
    for (int m = 0; m < 4; ++m) { acc[m][0] = f32x16{}; acc[m][1] = f32x16{}; }
    auto stA = [&](int pr, int i) { glds16(Ab + (size_t)(KV ? (pr & 7) : pr) * 128, voA[i], lds + (pr & 1) * 32768 + (wid * 4 + i) * 1024); };
    auto stW = [&](int t, int i) { glds16(((KV && t >= 16) ? W1 : W0) + (size_t)(KV ? (t & 15) : t) * 32 * ldw_b, voW[i], lds + 65536 + (t & 3) * 16384 + (wid >> 1) * 4096 + ((wid & 1) * 2 + i) * 1024); };
    bf16x8 af[2][4], wf[2][2];
    auto rd = [&](int rs, int t, int par, int s, int i) {
        if (i < 2) { LAS const char* sw = lds + (t & 3) * 16384; wf[rs][i] = cat8(lds_tr(sw + boff[i] + s * 2048), lds_tr(sw + boff[i] + s * 2048 + 512)); }
        else af[rs][i - 2] = lds_b128(lds + ((t >> 1) & 1) * 32768 + aoff[2 * par + s] + (i - 2) * 4096);
    };
    auto ks0 = [&](auto OT, int t, int par) {
#pragma unroll
        for (int i = 0; i < 8; ++i) { const int m = i >> 1, n = i & 1;
            acc[m][n] = OT.value ? mfma32(wf[0][n], af[0][m], acc[m][n]) : mfma32(af[0][m], wf[0][n], acc[m][n]);
            if (i < 6) rd(1, t, par, 1, i);
            SBAR(); }
    };
    auto ks1 = [&](auto OT, int t, int par, bool ld) {
#pragma unroll
        for (int i = 0; i < 8; ++i) { const int m = i >> 1, n = i & 1;
            acc[m][n] = OT.value ? mfma32(wf[1][n], af[1][m], acc[m][n]) : mfma32(af[1][m], wf[1][n], acc[m][n]);
            if (i < 6) rd(0, t + 1, par ^ 1, 0, i);
            if (ld) { if (par) { if (i >= 1 && i <= 4) stA((t + 3) >> 1, i - 1); if (i >= 5 && i <= 6) stW(t + 4, i - 5); } else if (i == 2 || i == 5) stW(t + 4, i == 5); }
            SBAR(); }
    };
    using TT = std::integral_constant<bool, true>; using FF = std::integral_constant<bool, false>;
#pragma unroll
    for (int pr = 0; pr < 2; ++pr) {
#pragma unroll
        for (int i = 0; i < 4; ++i) stA(pr, i);
        stW(2 * pr, 0); stW(2 * pr, 1); stW(2 * pr + 1, 0); stW(2 * pr + 1, 1); }
    WAIT_BAR(10);
#pragma unroll
    for (int i = 0; i < 6; ++i) rd(0, 0, 0, 0, i);
#pragma unroll 1
    for (int t = 0; t < 12; t += 2) {
        ks0(TT{}, t, 0);     WAIT_BAR(8); ks1(TT{}, t, 0, true);
        ks0(TT{}, t + 1, 1); WAIT_BAR(4); ks1(TT{}, t + 1, 1, true);
    }
    ks0(TT{}, 12, 0); WAIT_BAR(8);
    l2pf(warm, (lane < 24 ? lane : 0) * 128, lds + 131072 + wid * 256); l2pf(warm2, w2off, lds + 131072 + wid * 256);
    ks1(TT{}, 12, 0, true);
    ks0(TT{}, 13, 1); WAIT_BAR(6);  ks1(TT{}, 13, 1, true);
    ks0(TT{}, 14, 0); WAIT_BAR(10); ks1(TT{}, 14, 0, true);
    ks0(TT{}, 15, 1); WAIT_BAR(4);  ks1(TT{}, 15, 1, true);
    if constexpr (!KV) {
#pragma unroll 1
        for (int t = 16; t < 28; t += 2) {
            ks0(TT{}, t, 0);     WAIT_BAR(8); ks1(TT{}, t, 0, true);
            ks0(TT{}, t + 1, 1); WAIT_BAR(4); ks1(TT{}, t + 1, 1, true);
        }
        ks0(TT{}, 28, 0); WAIT_BAR(8);  ks1(TT{}, 28, 0, false);
        ks0(TT{}, 29, 1); WAIT_BAR(2);  mid(0); mid(1); ks1(TT{}, 29, 1, false);
        ks0(TT{}, 30, 0); WAIT_BAR(16); ks1(TT{}, 30, 0, false);
        ks0(TT{}, 31, 1); ks1(TT{}, 30, 0, false);
    } else {
        mid(0);
#pragma unroll
        for (int m = 0; m < 4; ++m) { acc[m][0] = f32x16{}; acc[m][1] = f32x16{}; }
        ks0(FF{}, 16, 0); WAIT_BAR(24); ks1(FF{}, 16, 0, true);
        ks0(FF{}, 17, 1); WAIT_BAR(20); ks1(FF{}, 17, 1, true);
        ks0(FF{}, 18, 0); WAIT_BAR(24); ks1(FF{}, 18, 0, true);
        ks0(FF{}, 19, 1); WAIT_BAR(4);  ks1(FF{}, 19, 1, true);
#pragma unroll 1
        for (int t = 20; t < 28; t += 2) {
            ks0(FF{}, t, 0);     WAIT_BAR(8); ks1(FF{}, t, 0, true);
            ks0(FF{}, t + 1, 1); WAIT_BAR(4); ks1(FF{}, t + 1, 1, true);
        }
        ks0(FF{}, 28, 0); WAIT_BAR(8); ks1(FF{}, 28, 0, false);
        ks0(FF{}, 29, 1); WAIT_BAR(2); ks1(FF{}, 29, 1, false);
        ks0(FF{}, 30, 0); WAIT_BAR(0); ks1(FF{}, 30, 0, false);
        ks0(FF{}, 31, 1); ks1(FF{}, 30, 0, false);
    }
}
constexpr int KVP1 = 65536;

struct KvUnit {
    bf16x8 xf[8];
    DI void issue(int part, LAS char* lds, const P1& p, int tb, int isv, int wid, int lane) {
        const char* wsrc = (const char*)(isv ? p.wvA : p.wkA) + wid * 8192;
        glds16x4(wsrc + part * 4096, lane * 16, lds + part * KVP1 + wid * 4096);
        if (part) {
#pragma unroll
            for (int s = 0; s < 8; ++s) xf[s] = *(const bf16x8*)((const char*)p.xA + (size_t)(32 * tb + (lane & 31)) * 2048 + (128 * wid + 16 * s + 8 * (lane >> 5)) * 2);
        }
    }
    template <class St>
    DI void finish(LAS char* lds, const P1& p, int tb, int isv, int wid, int lane, St st) {
        const int h = lane >> 5, blk = (lane >> 4) & 1, q = (lane & 15) >> 2, pq = lane & 3;
        st(0); st(1); SBAR();
        asm volatile("s_waitcnt vmcnt(4)" ::: "memory");
        f32x16 ak = f32x16{};
#pragma unroll
        for (int s = 0; s < 8; ++s) {
            if (s < 6) st(s + 2);
            LAS const char* b0 = lds + (s >> 2) * KVP1 + wid * 4096 + (16 * (s & 3) + 8 * h + q) * 64 + 32 * blk + 8 * pq;
            const bf16x8 wf = cat8(lds_tr(b0), lds_tr(b0 + 256));
            if (isv) ak = mfma32(xf[s], wf, ak); else ak = mfma32(wf, xf[s], ak);
        }
        asm volatile("s_waitcnt lgkmcnt(0)" ::: "memory");
#pragma unroll
        for (int i = 0; i < 4; ++i) *(LAS f32x4*)(lds + wid * 4096 + i * 1024 + lane * 16) = (f32x4){ak[4 * i], ak[4 * i + 1], ak[4 * i + 2], ak[4 * i + 3]};
        asm volatile("s_waitcnt lgkmcnt(0)\n\ts_barrier" ::: "memory");
        if (wid < 2) {
            f32x4 s0 = {0.f, 0.f, 0.f, 0.f}, s1 = {0.f, 0.f, 0.f, 0.f};
#pragma unroll
            for (int w = 0; w < 8; ++w) { s0 += *(LAS const f32x4*)(lds + w * 4096 + (2 * wid) * 1024 + lane * 16); s1 += *(LAS const f32x4*)(lds + w * 4096 + (2 * wid + 1) * 1024 + lane * 16); }
            u32x4 o; o.x = cvtpk(s0[0], s0[1]); o.y = cvtpk(s0[2], s0[3]); o.z = cvtpk(s1[0], s1[1]); o.w = cvtpk(s1[2], s1[3]);
            *(u32x4*)(p.ws + (isv ? WS_VA : WS_KA) + (size_t)(tb * 2 + wid) * 1024 + lane * 16) = o;
        }
    }
};

__global__ __launch_bounds__(512, 2) void k_proj(P1 p) {
    extern __shared__ __attribute__((aligned(16))) char smem[];
    LAS char* lds = (LAS char*)smem;
    const int tid = threadIdx.x, wid = __builtin_amdgcn_readfirstlane(tid >> 6), lane = tid & 63;
    const int bid = blockIdx.x, xcd = bid & 7, idx = bid >> 3;
    STAMP(p.ws, 0, 0);
    CODE_PF("_Z6k_proj2P1", "_Z6k_attn2P2-_Z6k_proj2P1", wid, lds + 131072 + wid * 256);
    if (bid == 0 && tid < 128) ((unsigned*)(p.ws + WS_FLAG))[tid * 16] = 0u;
    const int wr = wid >> 2, wc = wid & 3;
    const int pm = idx & 7, pn = 2 * xcd + ((idx >> 3) & 1), kind = idx >> 4;
    const int gw = bid * 8 + wid;
    const char* warm = gw < 1365 ? (const char*)p.xA + (size_t)gw * 3072 : (const char*)p.wqA + (size_t)(gw - 1365 < 681 ? gw - 1365 : 681) * 3072;
    f32x16 acc[4][2];
    auto store_f = [&](char* base, float sc) {
#pragma unroll
        for (int m = 0; m < 4; ++m)
#pragma unroll
            for (int n = 0; n < 2; ++n) { const int tb = pm * 8 + wr * 4 + m, fb = pn * 8 + wc * 2 + n, hd = fb >> 2, db = fb & 3;
                char* dst = base + (size_t)((hd * NTB + tb) * 8 + 2 * db) * 1024 + lane * 16;
                st16(dst, pack8(acc[m][n], 0, sc)); st16(dst + 1024, pack8(acc[m][n], 8, sc)); }
    };
    if (kind == 0) {
        KvUnit kv; const int tbk = xcd * 8 + pm, isv = (idx >> 3) & 1;
        big_stream<false>(lds, (const char*)p.xB + (size_t)pm * 256 * 2048, 2048, (const char*)p.wqB + pn * 512, nullptr, acc,
                          [&](int part) { kv.issue(part, lds, p, tbk, isv, wid, lane); }, warm, (const char*)(isv ? p.wvA : p.wkA) + wid * 8192, lane * 128, wid, lane);
        SBAR();
        kv.finish(lds, p, tbk, isv, wid, lane, [&](int g) { const int m = g >> 1, n = g & 1, tb = pm * 8 + wr * 4 + m, fb = pn * 8 + wc * 2 + n, hd = fb >> 2, db = fb & 3;
            char* dst = p.ws + WS_QB + (size_t)((hd * NTB + tb) * 8 + 2 * db) * 1024 + lane * 16;
            st16(dst, pack8(acc[m][n], 0, CB)); st16(dst + 1024, pack8(acc[m][n], 8, CB)); });
    } else {
        unsigned long long pc; int oa, oo;
        asm volatile("s_getpc_b64 %0\n.Lpc_anchor_%=:\n\ts_mov_b32 %1, _Z6k_attn2P2-.Lpc_anchor_%=\n\ts_mov_b32 %2, _Z5k_out2P3-.Lpc_anchor_%=" : "=s"(pc), "=s"(oa), "=s"(oo));
        const unsigned long long ca = pc + (long long)oa, co = pc + (long long)oo;
        const long lim = (long)(co - ca) + 4096 - 128;
        const char* w2 = (const char*)ca; unsigned w2off = (wid & 3) * 8192 + lane * 128; if (lim < 0) w2off = 0; else if ((long)w2off > lim) w2off = (unsigned)lim;
        big_stream<true>(lds, (const char*)p.cKV + (size_t)pm * 256 * 1024, 1024, (const char*)p.wukB + pn * 512, (const char*)p.wuvB + pn * 512, acc,
                         [&](int) { store_f(p.ws + WS_KB, 1.f); }, warm, w2, w2off, wid, lane);
#pragma unroll
        for (int m = 0; m < 4; ++m)
#pragma unroll
            for (int n = 0; n < 2; ++n) { const int tb = pm * 8 + wr * 4 + m, fb = pn * 8 + wc * 2 + n, hd = fb >> 2, db = fb & 3;
                char* dst = p.ws + WS_VB + (size_t)((hd * NTB + tb) * 8 + db) * 1024 + lane * 16;
                st16(dst, pack8(acc[m][n], 0, 1.f)); st16(dst + 4096, pack8(acc[m][n], 8, 1.f)); }
    }
    STAMP(p.ws, 0, 1); STAMP(p.ws, 0, 2); STAMP(p.ws, 0, 3);
}

struct P2 { const u16 *xA, *wqA, *woA, *woB; char* ws; };

DI float max16(const f32x16& s) {
    float m = fmaxf(s[0], s[1]);
#pragma unroll
    for (int i = 2; i < 16; ++i) m = fmaxf(m, s[i]);
    return m;
}
DI void diag_mask(f32x16& s, int r, int h) {
#pragma unroll
    for (int i = 0; i < 16; ++i) { const int kr = (i & 3) + 8 * (i >> 2) + 4 * h; if (kr > r) s[i] = -__builtin_inff(); }
}
DI bf16x8 pfrag(const unsigned (&pk)[8], int b) { u32x4 w = {pk[b], pk[b + 1], pk[b + 2], pk[b + 3]}; return __builtin_bit_cast(bf16x8, w); }

DI void attnB(LAS char* lds, char* ws, int hd, int xb, int wid, int lane) {
    const int r = lane & 31, h = lane >> 5;
    const int tbq = 4 * xb + wid, NT = 2 * (xb + 1);
    const char* kbase = ws + WS_KB + (size_t)hd * NTB * 8192 + wid * 4096;
    const char* vbase = ws + WS_VB + (size_t)hd * NTB * 8192 + wid * 4096;
    const unsigned lds0 = (unsigned)(size_t)lds + wid * 4096, lane16 = lane * 16;
    auto piece = [&](const char* src, unsigned m0v, int i) { if (i == 0) glds16o<0>(src, lane16, m0v); else if (i == 1) glds16o<1024>(src, lane16, m0v); else if (i == 2) glds16o<2048>(src, lane16, m0v); else glds16o<3072>(src, lane16, m0v); };
    glds16x4(kbase, lane * 16, lds + wid * 4096); glds16x4(vbase, lane * 16, lds + 32768 + wid * 4096); glds16x4(kbase + 16384, lane * 16, lds + 16384 + wid * 4096);
    bf16x8 qf[8];
#pragma unroll
    for (int s = 0; s < 8; ++s) qf[s] = *(const bf16x8*)(ws + WS_QB + (size_t)((hd * NTB + tbq) * 8 + s) * 1024 + lane * 16);
    f32x16 o[4], negm, S0, S1; float l = 0.f, t0, t1;
    bf16x8 fr[8]; unsigned pk[8], pn[4];
#pragma unroll
    for (int d = 0; d < 4; ++d) o[d] = f32x16{};
    LAS const char* lbase = lds + lane * 16;
    auto kaddr = [&](int j) { return lbase + ((j >> 1) & 1) * 16384 + (j & 1) * 8192; };
    auto vaddr = [&](int j) { return lbase + 32768 + ((j >> 1) & 1) * 16384 + (j & 1) * 8192; };
    auto slotA = [&](auto ST, f32x16& Sc, f32x16& Sn, int j, const char* src, unsigned m0v) {
        LAS const char* va = vaddr(j);
        float ea = 0.f, eb = 0.f;
#pragma unroll
        for (int i = 0; i < 4; ++i) pk[i] = pn[i];
#pragma unroll
        for (int i = 0; i < 8; ++i) {
            Sn = mfma32(fr[i], qf[i], i ? Sn : negm);
            fr[i] = lds_b128(va + i * 1024);
            if (!(i & 1)) { ea = __builtin_amdgcn_exp2f(Sc[8 + i]); eb = __builtin_amdgcn_exp2f(Sc[9 + i]); PIN(ea); PIN(eb); if (ST.value) piece(src, m0v, i >> 1); }
            else { pk[4 + (i >> 1)] = cvtpk(ea, eb); t0 += ea; t1 += eb; PIN(t0); PIN(t1); }
            SBAR();
        }
        float ts = t0 + t1;
        if (__builtin_expect(__any(!(ts < 1.f)), 0)) {
            const float delta = fmaxf(swap32_max(max16(Sc)) + SM_BIAS, 0.f), alpha = __builtin_amdgcn_exp2f(-delta);
            l *= alpha;
#pragma unroll
            for (int d = 0; d < 4; ++d) o[d] *= alpha;
            negm -= delta; Sn -= delta; t0 = 0.f; t1 = 0.f;
#pragma unroll
            for (int i = 0; i < 8; ++i) { const float a = __builtin_amdgcn_exp2f(Sc[2 * i] - delta), b = __builtin_amdgcn_exp2f(Sc[2 * i + 1] - delta); pk[i] = cvtpk(a, b); t0 += a; t1 += b; }
            ts = t0 + t1;
        }
        l += ts;
        if (j + 1 == tbq) diag_mask(Sn, r, h);
    };
    auto slotB = [&](auto ST, f32x16& Sn, int j, const char* src, unsigned m0v) {
        LAS const char* ka = kaddr(j + 2);
        float ea = 0.f, eb = 0.f;
        t0 = 0.f; t1 = 0.f;
#pragma unroll
        for (int i = 0; i < 8; ++i) {
            o[i & 3] = mfma32(fr[i], pfrag(pk, 4 * (i >> 2)), o[i & 3]);
            fr[i] = lds_b128(ka + i * 1024);
            if (!(i & 1)) { if (ST.value) piece(src, m0v, i >> 1); ea = __builtin_amdgcn_exp2f(Sn[i]); eb = __builtin_amdgcn_exp2f(Sn[i + 1]); PIN(ea); PIN(eb); }
            else { pn[i >> 1] = cvtpk(ea, eb); t0 += ea; t1 += eb; PIN(pn[i >> 1]); PIN(t0); PIN(t1); }
            SBAR();
        }
    };
    using TT = std::integral_constant<bool, true>; using FF = std::integral_constant<bool, false>;
    WAIT_BAR(0);
    {
        S0 = f32x16{};
#pragma unroll
        for (int s = 0; s < 8; ++s) fr[s] = lds_b128(lbase + s * 1024);
#pragma unroll
        for (int s = 0; s < 8; ++s) S0 = mfma32(fr[s], qf[s], S0);
#pragma unroll
        for (int s = 0; s < 8; ++s) fr[s] = lds_b128(lbase + 8192 + s * 1024);
        if (tbq == 0) diag_mask(S0, r, h);
        const float nm = -(swap32_max(max16(S0)) + SM_BIAS);
#pragma unroll
        for (int i = 0; i < 16; ++i) negm[i] = nm;
        S0 += nm;
        t0 = 0.f; t1 = 0.f;
#pragma unroll
        for (int i = 0; i < 4; ++i) { const float a = __builtin_amdgcn_exp2f(S0[2 * i]), b = __builtin_amdgcn_exp2f(S0[2 * i + 1]); pn[i] = cvtpk(a, b); t0 += a; t1 += b; }
    }
    for (int j = 0;; j += 2) {
        const int t = j >> 1;
        WAIT_BAR(0);
        if ((t & 3) != 3) __builtin_amdgcn_s_setprio(2); else __builtin_amdgcn_s_setprio(0);
        const int tk = t + 2 < NT ? t + 2 : NT - 1, tv = t + 1 < NT ? t + 1 : NT - 1;
        slotA(TT{}, S0, S1, j, kbase + (size_t)tk * 16384, lds0 + (t & 1) * 16384);  slotB(TT{}, S1, j, vbase + (size_t)tv * 16384, lds0 + 32768 + ((t + 1) & 1) * 16384);
        if (j == tbq) break;
        slotA(FF{}, S1, S0, j + 1, nullptr, 0u); slotB(FF{}, S0, j + 1, nullptr, 0u);
        if (j + 1 == tbq) break;
    }
    if (wid < 2) WAIT_BAR(0);
    asm volatile("s_waitcnt vmcnt(0)" ::: "memory");
    const float inv = 1.f / swap32_add(l);
    char* dst = ws + WS_CTX + (size_t)(tbq * 256 + hd * 8) * 1024 + lane * 16;
#pragma unroll
    for (int d = 0; d < 4; ++d) { st16(dst + (2 * d) * 1024, pack8(o[d], 0, inv)); st16(dst + (2 * d + 1) * 1024, pack8(o[d], 8, inv)); }
}

DI void projA(LAS char* lds, const P2& p, int hg, int tg, bf16x8 (&qf)[2][2], int wid, int lane) {
    constexpr int STG = 24576, NST = 16;
    const int r = lane & 31, h = lane >> 5;
    const char* xsrc = (const char*)p.xA + (size_t)tg * 64 * 2048;
    unsigned voX[2], voW[4];
#pragma unroll
    for (int i = 0; i < 2; ++i) { const int row = (wid * 2 + i) * 8 + (lane >> 3), ch = (lane & 7) ^ ((row >> 1) & 7); voX[i] = (unsigned)row * 2048u + ch * 16; }
#pragma unroll
    for (int i = 0; i < 4; ++i) { const int k = ((wid & 1) * 4 + i) * 8 + (lane >> 3), slot = lane & 7;
        voW[i] = (unsigned)k * 2048u + (128 * hg + 64 * (wid >> 1) + 32 * ((slot >> 2) ^ ((k >> 1) & 1)) + 8 * (slot & 3)) * 2; }
    auto stage1 = [&](int slot, int t, int i) {
        LAS char* base = lds + slot * STG;
        if (i < 2) glds16(xsrc + (size_t)t * 128, voX[i], base + (wid * 2 + i) * 1024);
        else glds16((const char*)p.wqA + (size_t)t * 64 * 2048, voW[i - 2], base + 8192 + (wid >> 1) * 8192 + ((wid & 1) * 4 + i - 2) * 1024);
    };
    int aoff[4];
#pragma unroll
    for (int s = 0; s < 4; ++s) aoff[s] = r * 128 + (((2 * s + h) ^ ((r >> 1) & 7)) << 4);
    const int blk = (lane >> 4) & 1, q = (lane & 15) >> 2, pq = lane & 3;
    const int boff = 8192 + (wid >> 1) * 8192 + (8 * h + q) * 128 + 64 * ((wid & 1) ^ (q >> 1)) + 32 * blk + 8 * pq;
    f32x16 acc[2]; acc[0] = f32x16{}; acc[1] = f32x16{};
    bf16x8 wf[4], a0[4], a1[4];
    auto rd = [&](int slot, int s) { LAS const char* sa = lds + slot * STG;
        wf[s] = cat8(lds_tr(sa + boff + s * 2048), lds_tr(sa + boff + s * 2048 + 512)); a0[s] = lds_b128(sa + aoff[s]); a1[s] = lds_b128(sa + aoff[s] + 4096); };
#pragma unroll
    for (int t = 0; t < 3; ++t)
#pragma unroll
        for (int i = 0; i < 6; ++i) stage1(t, t, i);
    WAIT_BAR(12);
    rd(0, 0); rd(0, 1);
    int slot = 0;
    for (int t = 0; t < NST; ++t) {
        int sn = slot + 1; if (sn == 3) sn = 0;
        rd(slot, 2); acc[0] = mfma32(wf[0], a0[0], acc[0]); acc[1] = mfma32(wf[0], a1[0], acc[1]); SBAR();
        rd(slot, 3); acc[0] = mfma32(wf[1], a0[1], acc[0]); acc[1] = mfma32(wf[1], a1[1], acc[1]); SBAR();
        if (t + 2 < NST) { WAIT_BAR(6); } else { WAIT_BAR(0); }
        rd(sn, 0); acc[0] = mfma32(wf[2], a0[2], acc[0]); acc[1] = mfma32(wf[2], a1[2], acc[1]); SBAR();
        if (t + 3 < NST) { stage1(slot, t + 3, 0); stage1(slot, t + 3, 1); stage1(slot, t + 3, 2); }
        rd(sn, 1); acc[0] = mfma32(wf[3], a0[3], acc[0]); acc[1] = mfma32(wf[3], a1[3], acc[1]); SBAR();
        if (t + 3 < NST) { stage1(slot, t + 3, 3); stage1(slot, t + 3, 4); stage1(slot, t + 3, 5); }
        slot = sn;
    }
#pragma unroll
    for (int u = 0; u < 2; ++u)
#pragma unroll
        for (int s = 0; s < 2; ++s) qf[u][s] = __builtin_bit_cast(bf16x8, pack8(acc[u], 8 * s, CA));
    WAIT_BAR(0);
}
DI void attnA(LAS char* lds, const P2& p, int hg, int tg, int wid, int lane) {
    char* ws = p.ws;
    const int hd = 4 * hg + wid, tb0 = 2 * tg;
    bf16x8 qf[2][2];
    __builtin_amdgcn_s_setprio(3);
    projA(lds, p, hg, tg, qf, wid, lane);
    STAMP(ws, 1, 1);
    __builtin_amdgcn_s_setprio(1);
    STAMP(ws, 1, 2);
    const char* kvsrc = ws + ((wid & 2) ? WS_VA : WS_KA) + (wid & 1) * 4096;
    auto stage = [&](int slot, int t) { glds16x4(kvsrc + (size_t)t * 8192, lane * 16, lds + slot * 16384 + wid * 4096); };
    stage(0, 0); stage(1, 1);
    f32x16 o[2], negm[2], Sa[2], Sb[2]; float l[2] = {0.f, 0.f};
    o[0] = f32x16{}; o[1] = f32x16{};
    bf16x8 kf[2], vf[2]; unsigned pp[2][8];
    LAS const char* lbase = lds + lane * 16;
    auto step = [&](f32x16 (&Sc)[2], f32x16 (&Sn)[2], LAS const char* ka, LAS const char* va) {
        unsigned pn[2][8]; float t[2][2] = {{0.f, 0.f}, {0.f, 0.f}}, e[2][4];
#pragma unroll
        for (int i = 0; i < 9; ++i) {
            if (i < 8) {
                const int u = i & 1, s = (i >> 1) & 1;
                if (i < 4) Sn[u] = mfma32(kf[s], qf[u][s], s ? Sn[u] : negm[u]);
                else o[u] = mfma32(vf[s], pfrag(pp[u], 4 * s), o[u]);
                if (i == 3) { kf[0] = lds_b128(ka); kf[1] = lds_b128(ka + 1024); }
                if (i == 7) { vf[0] = lds_b128(va); vf[1] = lds_b128(va + 1024); }
#pragma unroll
                for (int k = 0; k < 4; ++k) { e[i & 1][k] = __builtin_amdgcn_exp2f(Sc[i >> 2][(i & 3) * 4 + k]); PIN(e[i & 1][k]); }
            }
            if (i > 0) {
                const int g = i - 1, uu = g >> 2, w = (g & 3) * 2; const float* x = e[g & 1];
                pn[uu][w] = cvtpk(x[0], x[1]); pn[uu][w + 1] = cvtpk(x[2], x[3]);
                t[uu][0] += x[0]; t[uu][1] += x[1]; t[uu][0] += x[2]; t[uu][1] += x[3]; PIN(t[uu][0]); PIN(t[uu][1]); PIN(pn[uu][w]); PIN(pn[uu][w + 1]);
            }
            if (i < 8) SBAR();
        }
        float ts[2] = {t[0][0] + t[0][1], t[1][0] + t[1][1]};
        if (__builtin_expect(__any(!(ts[0] + ts[1] < 1.f)), 0)) {
#pragma unroll
            for (int u = 0; u < 2; ++u) {
                const float dl = fmaxf(swap32_max(max16(Sc[u])) + SM_BIAS, 0.f), al = __builtin_amdgcn_exp2f(-dl);
                l[u] *= al; o[u] *= al; negm[u] -= dl; Sn[u] -= dl; float a0 = 0.f, a1 = 0.f;
#pragma unroll
                for (int i = 0; i < 8; ++i) { const float a = __builtin_amdgcn_exp2f(Sc[u][2 * i] - dl), b = __builtin_amdgcn_exp2f(Sc[u][2 * i + 1] - dl); pn[u][i] = cvtpk(a, b); a0 += a; a1 += b; }
                ts[u] = a0 + a1;
            }
        }
#pragma unroll
        for (int u = 0; u < 2; ++u) { l[u] += ts[u];
#pragma unroll
            for (int i = 0; i < 8; ++i) pp[u][i] = pn[u][i]; }
    };
    WAIT_BAR(4);
    {
        kf[0] = lds_b128(lbase); kf[1] = lds_b128(lbase + 1024);
#pragma unroll
        for (int u = 0; u < 2; ++u) {
            Sa[u] = mfma32(kf[0], qf[u][0], f32x16{}); Sa[u] = mfma32(kf[1], qf[u][1], Sa[u]);
            const float nm = -(swap32_max(max16(Sa[u])) + SM_BIAS);
#pragma unroll
            for (int i = 0; i < 16; ++i) negm[u][i] = nm;
            Sa[u] += nm;
#pragma unroll
            for (int i = 0; i < 8; ++i) pp[u][i] = 0u;
        }
        kf[0] = lds_b128(lbase + 2048); kf[1] = lds_b128(lbase + 2048 + 1024);
        vf[0] = kf[0]; vf[1] = kf[1];
    }
    int slot = 0;
    for (int t = 0; t < 16; ++t) {
        WAIT_BAR(0);
        int sn = slot + 1; if (sn == 3) sn = 0; int sp = sn + 1; if (sp == 3) sp = 0;
        if (t + 2 < 16) stage(sp, t + 2);
        if (t == 2) { const int gw = (hg * 32 + tg) * 4 + wid; l2pf((const char*)p.woB + (size_t)gw * 8192, lane * 128, lds + 49152 + wid * 256);
                      l2pf((const char*)p.woA + (size_t)gw * 2048, (lane & 15) * 128, lds + 49152 + wid * 256); }
        LAS const char* cur = lbase + slot * 16384; LAS const char* nxt = lbase + sn * 16384;
        step(Sa, Sb, cur + 2 * 2048, cur + 8192);
        step(Sb, Sa, cur + 3 * 2048, cur + 8192 + 2048);
        step(Sa, Sb, nxt, cur + 8192 + 2 * 2048);
        step(Sb, Sa, nxt + 2048, cur + 8192 + 3 * 2048);
        slot = sn;
    }
#pragma unroll
    for (int s = 0; s < 2; ++s)
#pragma unroll
        for (int u = 0; u < 2; ++u) o[u] = mfma32(vf[s], pfrag(pp[u], 4 * s), o[u]);
#pragma unroll
    for (int u = 0; u < 2; ++u) {
        const float inv = 1.f / swap32_add(l[u]);
        char* dst = ws + WS_OA + (size_t)((tb0 + u) * 64 + hd * 2) * 1024 + lane * 16;
        st16(dst, pack8(o[u], 0, inv)); st16(dst + 1024, pack8(o[u], 8, inv));
    }
}

__global__ __launch_bounds__(256, 2) void k_attn(P2 p) {
    extern __shared__ __attribute__((aligned(16))) char smem[];
    LAS char* lds = (LAS char*)smem;
    const int tid = threadIdx.x, wid = __builtin_amdgcn_readfirstlane(tid >> 6), lane = tid & 63;
    const int bid = blockIdx.x;
    const int xcd = bid & 7, idx = bid >> 3,
        kind = (idx ^ (idx >> 5)) & 1,
        it = (idx >> 1) & 15 | (idx >> 5) << 4;
    STAMP(p.ws, 1, 0);
    CODE_PF("_Z6k_attn2P2", "_Z5k_out2P3-_Z6k_attn2P2", wid, lds + 73728 + wid * 256);
    if (kind == 0) { attnA(lds, p, xcd, it, wid, lane); }
    else {
        const int hd = xcd + 8 * (it >> 3), x = it & 7;
        attnB(lds, p.ws, hd, 15 - x, wid, lane);
        WAIT_BAR(0);
        STAMP(p.ws, 1, 1);
        attnB(lds, p.ws, hd, x, wid, lane); STAMP(p.ws, 1, 2); }
    STAMP(p.ws, 1, 3);
}

struct P3 { const u16 *woA, *woB; char* ws; u16* out; };

constexpr int NT1 = 24;
constexpr int PUBD = 4, POLLD = 5;
DI void out_store(LAS char*, u16* out, int tb0, int col0, const f32x16 (&acc)[2][2], int wid, int lane) {
    const int wm = wid >> 1, wn = wid & 1, r = lane & 31, h = lane >> 5;
#pragma unroll
    for (int m = 0; m < 2; ++m) {
        char* dst = (char*)out + (size_t)((tb0 + 2 * wm + m) * 32 + r) * 4096 + (col0 + 64 * wn + 8 * h) * 2;
#pragma unroll
        for (int c = 0; c < 2; ++c) { *(u32x4*)(dst + c * 64) = pack8(acc[m][c], 0, 1.f); *(u32x4*)(dst + c * 64 + 32) = pack8(acc[m][c], 8, 1.f); }
    }
}

__global__ __launch_bounds__(512, 2) void k_out(P3 p) {
    extern __shared__ __attribute__((aligned(16))) char smem[];
    LAS char* lds = (LAS char*)smem;
    constexpr int STG = 32768, nt = 40;
    const int tid = threadIdx.x, wid = __builtin_amdgcn_readfirstlane(tid >> 6), lane = tid & 63;
    const int bid = blockIdx.x, xg = bid & 7, role = (xg >> 2) ^ 1, ti = bid >> 3;
    const int pm = 8 * ((xg >> 1) & 1) + (ti >> 2), pn = 4 * (xg & 1) + (ti & 3), tb0 = pm * 4, tau = pm * 8 + pn;
    STAMP(p.ws, 2, 0);
    if (wid >= 4) {
        CODE_PF("_Z5k_out2P3", "6144", 0, lds + 131072 + (wid - 4) * 256);
        const int lw = wid - 4;
        const char* a0 = p.ws + WS_CTX + (size_t)((tb0 + lw) * 256 + (role ? 4 * (64 - NT1) : 0)) * 1024;
        const char* w0 = (const char*)p.woB + pn * 256 + (size_t)(role ? 64 - NT1 : 0) * 64 * 2048;
        const char* a1 = p.ws + WS_OA + (size_t)((tb0 + lw) * 64) * 1024 - (size_t)NT1 * 4096;
        const char* w1 = (const char*)p.woA + pn * 256 - (size_t)NT1 * 64 * 2048;
        const int tsw = role ? NT1 : nt;
        unsigned voW[4];
#pragma unroll
        for (int i = 0; i < 4; ++i) { const int pc = 4 * lw + i, g = pc >> 3, k = (pc & 7) * 8 + (lane >> 3), slot = lane & 7;
            const int col = 64 * g + 32 * ((slot >> 2) ^ ((k >> 1) & 1)) + 8 * (slot & 3); voW[i] = (unsigned)k * 2048u + col * 2; }
        auto stage = [&](int t) {
            LAS char* base = lds + (t & 3) * STG;
            const char* as = (t < tsw ? a0 : a1) + (size_t)t * 4096; const char* wsd = (t < tsw ? w0 : w1) + (size_t)t * (64 * 2048);
            glds16x4(as, lane * 16, base + lw * 4096);
#pragma unroll
            for (int i = 0; i < 4; ++i) glds16(wsd, voW[i], base + 16384 + (4 * lw + i) * 1024);
        };
#pragma unroll
        for (int t = 0; t < 4; ++t) stage(t);
        WAIT_BAR(24);
        for (int t = 0; t < nt; ++t) {
            if (t + 3 < nt) { WAIT_BAR(16); } else if (t + 2 < nt) { WAIT_BAR(8); } else { WAIT_BAR(0); }
            if (t + 4 < nt) stage(t + 4);
        }
    } else {
        const int wm = wid >> 1, wn = wid & 1, h = lane >> 5;
        const int blk = (lane >> 4) & 1, q = (lane & 15) >> 2, pq = lane & 3, pp = ((pq & 1) << 1) | (pq >> 1);
        GAS unsigned* flag = (GAS unsigned*)(p.ws + WS_FLAG) + tau * 16;
        const __amdgpu_buffer_rsrc_t rs = __builtin_amdgcn_make_buffer_rsrc(p.ws + WS_PART + (size_t)tau * 65536, 0, 65536, 0x00020000);
        int boff[2];
#pragma unroll
        for (int c = 0; c < 2; ++c) boff[c] = 16384 + wn * 8192 + (4 * h + q) * 128 + 64 * (c ^ (q >> 1)) + 32 * blk + 8 * pp;
        const int aoff = (2 * wm * 4) * 1024 + lane * 16;
        f32x16 acc[2][2];
#pragma unroll
        for (int m = 0; m < 2; ++m) { acc[m][0] = f32x16{}; acc[m][1] = f32x16{}; }
        bf16x8 af[4][2], wf[4][2];
        auto rd = [&](int slot, int s, int i) {
            LAS const char* sa = lds + slot * STG;
            if (i & 1) af[s][i >> 1] = lds_b128(sa + aoff + ((i >> 1) * 4 + s) * 1024);
            else wf[s][i >> 1] = cat8(lds_tr(sa + boff[i >> 1] + s * 2048), lds_tr(sa + boff[i >> 1] + s * 2048 + 1024));
        };
        auto kstep = [&](int s, int nslot) {
#pragma unroll
            for (int i = 0; i < 4; ++i) {
                const int m = i >> 1, c = i & 1;
                acc[m][c] = mfma32(wf[s][c], af[s][m], acc[m][c]);
                rd(nslot, (s + 2) & 3, i);
                SBAR();
            }
        };
        auto tiles = [&](int t0, int t1) {
#pragma unroll 1
            for (int t = t0; t < t1; ++t) {
                kstep(0, t & 3); kstep(1, t & 3);
                asm volatile("s_waitcnt lgkmcnt(0)\n\ts_barrier" ::: "memory");
                kstep(2, (t + 1) & 3); kstep(3, (t + 1) & 3);
            }
        };
        WAIT_BAR(0);
#pragma unroll
        for (int i = 0; i < 4; ++i) { rd(0, 0, i); rd(0, 1, i); }
        if (role == 1) {
            tiles(0, NT1);
#pragma unroll
            for (int m = 0; m < 2; ++m)
#pragma unroll
                for (int c = 0; c < 2; ++c) {
#pragma unroll
                    for (int i = 0; i < 4; ++i) { u32x4 v;
#pragma unroll
                        for (int j = 0; j < 4; ++j) v[j] = __float_as_uint(acc[m][c][4 * i + j]);
                        __builtin_amdgcn_raw_buffer_store_b128(v, rs, wid * 16384 + ((m * 2 + c) * 4 + i) * 1024 + lane * 16, 0, 16); }
                    acc[m][c] = f32x16{}; }
            tiles(NT1, NT1 + PUBD);
            asm volatile("s_waitcnt vmcnt(0)" ::: "memory");
            tiles(NT1 + PUBD, NT1 + PUBD + 1);
            if (tid == 0) __hip_atomic_store(flag, 1u, __ATOMIC_RELAXED, __HIP_MEMORY_SCOPE_AGENT);
            STAMP(p.ws, 2, 1);
            tiles(NT1 + PUBD + 1, nt);
            STAMP(p.ws, 2, 2);
            out_store(lds + 32768 + wid * 8192, p.out, tb0, pn * 128, acc, wid, lane);
        } else {
            tiles(0, nt - POLLD);
            while ((unsigned)__builtin_amdgcn_readfirstlane(__hip_atomic_load(flag, __ATOMIC_RELAXED, __HIP_MEMORY_SCOPE_AGENT)) != 1u) __builtin_amdgcn_s_sleep(2);
            __builtin_amdgcn_fence(__ATOMIC_ACQUIRE, "wavefront");
            u32x4 pv[16];
#pragma unroll
            for (int i = 0; i < 16; ++i) pv[i] = __builtin_amdgcn_raw_buffer_load_b128(rs, wid * 16384 + i * 1024 + lane * 16, 0, 16);
            STAMP(p.ws, 2, 1);
            tiles(nt - POLLD, nt);
#pragma unroll
            for (int i = 0; i < 16; ++i)
#pragma unroll
                for (int j = 0; j < 4; ++j) acc[i >> 3][(i >> 2) & 1][4 * (i & 3) + j] += __uint_as_float(pv[i][j]);
            STAMP(p.ws, 2, 2);
            out_store(lds + 32768 + wid * 8192, p.out, tb0, 1024 + pn * 128, acc, wid, lane);
        }
        STAMP(p.ws, 2, 3);
    }
}

#define BENCH_MARK(s)
extern "C" void kernel_launch(void* const* d_in, const int* in_sizes, int n_in, void* d_out, int out_size, void* d_ws, size_t ws_size,
                              hipStream_t stream) {
    (void)in_sizes; (void)n_in; (void)out_size; (void)ws_size;
    (void)hipFuncSetAttribute((const void*)k_proj, hipFuncAttributeMaxDynamicSharedMemorySize, 133120);
    (void)hipFuncSetAttribute((const void*)k_attn, hipFuncAttributeMaxDynamicSharedMemorySize, 74752);
    (void)hipFuncSetAttribute((const void*)k_out, hipFuncAttributeMaxDynamicSharedMemorySize, 132096);
    P1 p1; p1.wqA = (const u16*)d_in[1]; p1.xA = (const u16*)d_in[0]; p1.wkA = (const u16*)d_in[2]; p1.wvA = (const u16*)d_in[3];
    p1.xB = (const u16*)d_in[5]; p1.cKV = (const u16*)d_in[6]; p1.wqB = (const u16*)d_in[7]; p1.wukB = (const u16*)d_in[8]; p1.wuvB = (const u16*)d_in[9];
    p1.ws = (char*)d_ws;
    BENCH_MARK(stream);
    k_proj<<<256, 512, 133120, stream>>>(p1);
    BENCH_MARK(stream);
    P2 p2; p2.woA = (const u16*)d_in[4]; p2.woB = (const u16*)d_in[10]; p2.xA = (const u16*)d_in[0]; p2.wqA = (const u16*)d_in[1]; p2.ws = (char*)d_ws;
    k_attn<<<512, 256, 74752, stream>>>(p2);
    BENCH_MARK(stream);
    P3 p3; p3.woA = (const u16*)d_in[4]; p3.woB = (const u16*)d_in[10]; p3.ws = (char*)d_ws; p3.out = (u16*)d_out;
    k_out<<<256, 512, 132096, stream>>>(p3);
    BENCH_MARK(stream);
}
```
